```python
import torch

def _softmax(x, axis=-1):
    return torch.softmax(x, dim=axis)
def _gelu(x):
    return torch.nn.functional.gelu(x, approximate="tanh")
def _silu(x):
    return torch.nn.functional.silu(x)
def _sigmoid(x):
    return torch.sigmoid(x)
def _layernorm(x, axis=-1, eps=1e-5):
    m = x.mean(dim=axis, keepdim=True)
    v = x.var(dim=axis, keepdim=True, unbiased=False)
    return (x - m) / torch.sqrt(v + eps)
def _scatter_add(tgt, idx, src, axis=0):
    return tgt.clone().index_add_(axis, idx, src)
def _segment_sum(src, seg, n):
    out = torch.zeros((n,) + src.shape[1:], dtype=src.dtype)
    return out.index_add_(0, seg, src)
def _pad(x, pw):
    flat = tuple(v for pair in reversed(pw) for v in pair)
    return torch.nn.functional.pad(x, flat)

kernel_name = 'recurrent_scan__ssm_conv_s4__dense_activation_mish__prompt'

def setup_inputs():

    _w = (lambda a: (lambda t: t.to(torch.bfloat16) if t.is_floating_point() else t)(torch.as_tensor(a)))
    def _A_setup():
        B, L, D, N = 1, 2048, 640, 128
        A_re = -(lambda _x: torch.exp(torch.as_tensor(_x)))((lambda *s: torch.randn(*s, dtype=torch.bfloat16) * 3.2)(D, N))
        A_im = torch.pi * (lambda *s: torch.randn(*s, dtype=torch.bfloat16) * 0.2)(D, N)
        B_re = (lambda *s: torch.randn(*s, dtype=torch.bfloat16) * 0.2)(D, N)
        B_im = (lambda *s: torch.randn(*s, dtype=torch.bfloat16) * 0.2)(D, N)
        C_re = (lambda *s: torch.randn(*s, dtype=torch.bfloat16) * 0.2)(D, N)
        C_im = (lambda *s: torch.randn(*s, dtype=torch.bfloat16) * 0.2)(D, N)
        log_dt = (lambda *s: torch.randn(*s, dtype=torch.bfloat16) * 0.2)(D) * 0.5 - 4.0
        Dskip = (lambda *s: torch.randn(*s, dtype=torch.bfloat16) * 0.2)(D)
        return {
            "u": _w((lambda *s: torch.randn(*s, dtype=torch.bfloat16) * 0.05)(B, L, D)),
            "A_re": _w(A_re),
            "A_im": _w(A_im),
            "B_re": _w(B_re),
            "B_im": _w(B_im),
            "C_re": _w(C_re),
            "C_im": _w(C_im),
            "log_dt": _w(log_dt),
            "Dskip": _w(Dskip),
        }
    _A_in = _A_setup()

    def _B_setup():
        B, S, D, F = 1, 2048, 640, 1728
        return {
            "x": _w((lambda *s: torch.randn(*s, dtype=torch.bfloat16) * 0.05)(B, S, D)),
            "w1": _w((lambda *s: torch.randn(*s, dtype=torch.bfloat16) * 0.8)(D, F) * (D ** -0.5)),
            "b1": _w((lambda *s: torch.randn(*s, dtype=torch.bfloat16) * 0.4)(F)),
            "w2": _w((lambda *s: torch.randn(*s, dtype=torch.bfloat16) * 0.1)(F, D) * (F ** -0.5)),
        }
    _B_in = _B_setup()

    return {
        "A_u": _A_in["u"],
        "A_A_re": _A_in["A_re"],
        "A_A_im": _A_in["A_im"],
        "A_B_re": _A_in["B_re"],
        "A_B_im": _A_in["B_im"],
        "A_C_re": _A_in["C_re"],
        "A_C_im": _A_in["C_im"],
        "A_log_dt": _A_in["log_dt"],
        "A_Dskip": _A_in["Dskip"],
        "B_x": _B_in["x"],
        "B_w1": _B_in["w1"],
        "B_b1": _B_in["b1"],
        "B_w2": _B_in["w2"],
    }

def reference(A_u, A_A_re, A_A_im, A_B_re, A_B_im, A_C_re, A_C_im, A_log_dt, A_Dskip, B_x, B_w1, B_b1, B_w2):
    def _A_ref(u, A_re, A_im, B_re, B_im, C_re, C_im, log_dt, Dskip):
        Bsz, L, D = u.shape
        dt = torch.exp(log_dt)[:, None]
        A = A_re + 1j * A_im
        Bc = B_re + 1j * B_im
        Cc = C_re + 1j * C_im
        logdA = dt * A
        dB = dt * Bc
        l = (lambda x, dt: x.to(dt))(torch.arange(L), A_re.dtype)
        P = torch.exp(logdA[:, :, None] * l[None, None, :])
        K = torch.einsum("dn,dnl,dn->dl", Cc, P, dB).real
        L2 = 2 * L
        Kp = _pad(K, ((0, 0), (0, L)))
        up = (lambda x, *d: x.permute(*d))(u, 0, 2, 1)
        up = _pad(up, ((0, 0), (0, 0), (0, L)))
        Kf = (lambda x, axes=(-2,-1): torch.fft.rfft2(x.float(), dim=axes))(Kp[:, None, :], axes=(-2, -1))
        Uf = (lambda x, axes=(-2,-1): torch.fft.rfft2(x.float(), dim=axes))(up[:, :, None, :], axes=(-2, -1))
        Yf = Uf * Kf[None, :, :, :]
        y = (lambda x, s, axes=(-2,-1): torch.fft.irfft2(x, s=s, dim=axes).to(torch.bfloat16))(Yf, (1, L2), axes=(-2, -1))[:, :, 0, :L]
        y = (lambda x, *d: x.permute(*d))(y, 0, 2, 1) + u * Dskip[None, None, :]
        return y

    def _B_ref(x, w1, b1, w2):
        h = torch.einsum("bsd,df->bsf", x, w1) + b1
        sp = torch.log1p(torch.exp(-torch.abs(h))) + torch.where(h > 0, h, torch.zeros_like(h))
        act = h * torch.tanh(sp)
        return torch.einsum("bsf,fd->bsd", act, w2)

    x = A_u
    xn = _layernorm(x, axis=-1)
    s = _A_ref(xn, A_A_re, A_A_im, A_B_re, A_B_im, A_C_re, A_C_im, A_log_dt, A_Dskip)
    g = _silu(_B_ref(xn, B_w1, B_b1, B_w2))
    return x + s * g
```

```cpp
#include <hip/hip_runtime.h>
#include <hip/hip_bf16.h>
#include <stdint.h>

typedef unsigned short u16;
#define EXACT_L 0

constexpr int L_ = 2048, D_ = 640, N_ = 128, F_ = 1728;

__device__ __forceinline__ float bf2f(u16 v) { return __uint_as_float(((unsigned)v) << 16); }
__device__ __forceinline__ u16 f2bf(float f) {
    unsigned u = __float_as_uint(f);
    u += 0x7FFFu + ((u >> 16) & 1u);
    return (u16)(u >> 16);
}
__device__ __forceinline__ float rbf(float f) { return bf2f(f2bf(f)); }

__global__ void ln_kernel(const u16* __restrict__ x, u16* __restrict__ xn) {
    int row = blockIdx.x;
    int t = threadIdx.x;
    float v[10];
    float s = 0;
    for (int i = 0; i < 10; ++i) { v[i] = bf2f(x[row * D_ + t + 64 * i]); s += v[i]; }
    for (int o = 32; o; o >>= 1) s += __shfl_xor(s, o);
    float mean = s / D_;
    float q = 0;
    for (int i = 0; i < 10; ++i) { float d = v[i] - mean; q += d * d; }
    for (int o = 32; o; o >>= 1) q += __shfl_xor(q, o);
    float var = q / D_;
    float mb = rbf(mean), vb = rbf(var);
    float den = rbf(sqrtf(rbf(vb + 1e-5f)));
    for (int i = 0; i < 10; ++i) xn[row * D_ + t + 64 * i] = f2bf(rbf(v[i] - mb) / den);
}

__global__ void kgen_kernel(const u16* Are, const u16* Aim, const u16* Bre, const u16* Bim, const u16* Cre, const u16* Cim,
                            const u16* logdt, float* K) {
    int d = blockIdx.x;
    __shared__ float lr[N_], li[N_], cr[N_], ci[N_];
    float dt = rbf(expf(bf2f(logdt[d])));
    for (int n = threadIdx.x; n < N_; n += blockDim.x) {
        float ar = bf2f(Are[d * N_ + n]), ai = bf2f(Aim[d * N_ + n]);
        float br = dt * bf2f(Bre[d * N_ + n]), bi = dt * bf2f(Bim[d * N_ + n]);
        float c_r = bf2f(Cre[d * N_ + n]), c_i = bf2f(Cim[d * N_ + n]);
        lr[n] = dt * ar; li[n] = dt * ai;
        cr[n] = c_r * br - c_i * bi;
        ci[n] = c_r * bi + c_i * br;
    }
    __syncthreads();
    for (int l = threadIdx.x; l < L_; l += blockDim.x) {
        float tl = EXACT_L ? (float)l : rbf((float)l);
        float acc = 0;
        for (int n = 0; n < N_; ++n) {
            float e = expf(lr[n] * tl);
            float sn, cs;
            sincosf(li[n] * tl, &sn, &cs);
            acc += e * (cr[n] * cs - ci[n] * sn);
        }
        K[d * L_ + l] = acc;
    }
}

__global__ void conv_kernel(const u16* xn, const float* K, const u16* Dskip, u16* s) {
    int d = blockIdx.x;
    __shared__ float sk[L_], su[L_];
    for (int i = threadIdx.x; i < L_; i += blockDim.x) { sk[i] = K[d * L_ + i]; su[i] = bf2f(xn[i * D_ + d]); }
    __syncthreads();
    float ds = bf2f(Dskip[d]);
    for (int t = threadIdx.x; t < L_; t += blockDim.x) {
        float acc = 0;
        for (int l = 0; l <= t; ++l) acc += sk[l] * su[t - l];
        float y = rbf(acc);
        s[t * D_ + d] = f2bf(y + rbf(su[t] * ds));
    }
}

__global__ void gemm1_kernel(const u16* xn, const u16* w1, const u16* b1, u16* act) {
    int row = blockIdx.y;
    int f = blockIdx.x * blockDim.x + threadIdx.x;
    __shared__ float sx[D_];
    for (int i = threadIdx.x; i < D_; i += blockDim.x) sx[i] = bf2f(xn[row * D_ + i]);
    __syncthreads();
    if (f >= F_) return;
    float acc = 0;
    for (int k = 0; k < D_; ++k) acc += sx[k] * bf2f(w1[k * F_ + f]);
    float h = rbf(rbf(acc) + bf2f(b1[f]));
    float e = __expf(fminf(h, 20.f));
    float n = e * (e + 2.f);
    float a = h * n / (n + 2.f);
    act[row * F_ + f] = f2bf(a);
}

__global__ void gemm2_kernel(const u16* act, const u16* w2, const u16* x, const u16* s, u16* out) {
    int row = blockIdx.y;
    int c = blockIdx.x * blockDim.x + threadIdx.x;
    __shared__ float sa[F_];
    for (int i = threadIdx.x; i < F_; i += blockDim.x) sa[i] = bf2f(act[row * F_ + i]);
    __syncthreads();
    if (c >= D_) return;
    float acc = 0;
    for (int k = 0; k < F_; ++k) acc += sa[k] * bf2f(w2[k * D_ + c]);
    float o = rbf(acc);
    float g = rbf(o / (1.f + __expf(-o)));
    float sv = bf2f(s[row * D_ + c]);
    out[row * D_ + c] = f2bf(bf2f(x[row * D_ + c]) + rbf(sv * g));
}

extern "C" void kernel_launch(void* const* d_in, const int* in_sizes, int n_in,
                              void* d_out, int out_size, void* d_ws, size_t ws_size,
                              hipStream_t stream) {
    const u16* x = (const u16*)d_in[0];
    char* ws = (char*)d_ws;
    u16* xn = (u16*)ws;
    float* K = (float*)(ws + (4 << 20));
    u16* s = (u16*)(ws + (12 << 20));
    u16* act = (u16*)(ws + (16 << 20));
    ln_kernel<<<L_, 64, 0, stream>>>(x, xn);
    kgen_kernel<<<D_, 256, 0, stream>>>((const u16*)d_in[1], (const u16*)d_in[2], (const u16*)d_in[3], (const u16*)d_in[4],
                                        (const u16*)d_in[5], (const u16*)d_in[6], (const u16*)d_in[7], K);
    conv_kernel<<<D_, 256, 0, stream>>>(xn, K, (const u16*)d_in[8], s);
    gemm1_kernel<<<dim3((F_ + 191) / 192, L_), 192, 0, stream>>>(xn, (const u16*)d_in[10], (const u16*)d_in[11], act);
    gemm2_kernel<<<dim3(D_ / 128, L_), 128, 0, stream>>>(act, (const u16*)d_in[12], x, s, (u16*)d_out);
}
```

```cpp
#include <hip/hip_runtime.h>
#include <stdint.h>
#include <type_traits>
#include <initializer_list>

typedef unsigned short u16;
typedef unsigned int u32;
typedef unsigned long long u64;
typedef __attribute__((ext_vector_type(8))) __bf16 bf16x8;
typedef __attribute__((ext_vector_type(2))) __bf16 bf16x2;
typedef __attribute__((ext_vector_type(8))) _Float16 f16x8;
typedef __attribute__((ext_vector_type(2))) _Float16 f16x2;
typedef __attribute__((ext_vector_type(4))) short s16x4;
typedef __attribute__((ext_vector_type(2))) u16 u16x2;
typedef __attribute__((ext_vector_type(4))) float f32x4;
typedef __attribute__((ext_vector_type(16))) float f32x16;
typedef __attribute__((ext_vector_type(2))) float f32x2;
typedef __attribute__((ext_vector_type(4))) u32 u32x4;
typedef __attribute__((ext_vector_type(2))) u32 u32x2;
typedef __attribute__((ext_vector_type(4), aligned(4))) u32 u32x4a;
#define LDSP __attribute__((address_space(3)))
#define DI __device__ __forceinline__
template <int N> using IC = std::integral_constant<int, N>;

#define MULTI 0

#define WARM 0
#define TLBP 0
constexpr int NM = 128;
constexpr u32 WS_EPOCH = 0, WS_RD = 1024, WS_XBF = 4096, WS_W2F = 5120, WS_SF = 8192, WS_ACTF = 16384, WS_STAT = 32768;
constexpr u32 WS_XB = 1u << 20, WS_ST = 4u << 20, WS_W2T = 8u << 20, WS_ACT = 12u << 20;
constexpr int LDS_BYTES = 81920;
constexpr int SPIN_CAP = 1 << 16;

struct IdxTab {
    u32 v[9 * 128];
    static constexpr int idx(int l) {
        if (l < 0) return 660;
        if (l < 256) return l;
        int sh = l < 512 ? 1 : (l < 1024 ? 2 : 3);
        int q = (l + ((1 << (sh - 1)) - 1) + ((l >> sh) & 1)) >> sh;
        return 128 * sh + q;
    }
    constexpr IdxTab() : v() {
        for (int k = 0; k < 9; ++k)
            for (int m = 0; m < 128; ++m)
                v[k * 128 + m] = (u32)(2 * idx(2047 - (16 * m + 2 * k))) | ((u32)(2 * idx(2047 - (16 * m + 2 * k + 1))) << 16);
    }
};
__device__ const IdxTab g_idx = IdxTab();

#if defined(__HIP_DEVICE_COMPILE__)
#pragma clang attribute push(__attribute__((target("no-packed-fp32-ops"))), apply_to = function)
#endif
DI float asf(u32 v) { return __builtin_bit_cast(float, v); }
DI u32 asu(float v) { return __builtin_bit_cast(u32, v); }
DI bool wall(bool p) { return __builtin_amdgcn_ballot_w64(p) == ~0ull; }
DI float bf2f(u32 v) { return asf(v << 16); }
DI u32 pk2(float a, float b) { f32x2 f = {a, b}; return __builtin_bit_cast(u32, __builtin_convertvector(f, bf16x2)); }
DI u32 pkh(float a, float b) { f32x2 f = {a, b}; return __builtin_bit_cast(u32, __builtin_convertvector(f, f16x2)); }
DI float rbf(float a) { return asf(pk2(a, a) << 16); }
DI float lo16(u32 v) { return asf(v << 16); }
DI float hi16(u32 v) { return asf(v & 0xFFFF0000u); }
DI u32 mktag(u32 e) { return e * 0x9E3779B1u + 0x7F4A7C15u; }
template <int CTRL, int RM>
DI float dpp(float v) { return asf((u32)__builtin_amdgcn_update_dpp(0, (int)asu(v), CTRL, RM, 0xf, true)); }
DI float wave_sum(float v) {
    v += dpp<0x111, 0xf>(v); v += dpp<0x112, 0xf>(v); v += dpp<0x114, 0xf>(v); v += dpp<0x118, 0xf>(v);
    v += dpp<0x142, 0xa>(v); v += dpp<0x143, 0xc>(v);
    return asf((u32)__builtin_amdgcn_readlane((int)asu(v), 63));
}
DI float wave_max(float v) {
    v = __builtin_fmaxf(v, dpp<0x111, 0xf>(v)); v = __builtin_fmaxf(v, dpp<0x112, 0xf>(v));
    v = __builtin_fmaxf(v, dpp<0x114, 0xf>(v)); v = __builtin_fmaxf(v, dpp<0x118, 0xf>(v));
    v = __builtin_fmaxf(v, dpp<0x142, 0xa>(v)); v = __builtin_fmaxf(v, dpp<0x143, 0xc>(v));
    return asf((u32)__builtin_amdgcn_readlane((int)asu(v), 63));
}

#define WAITV(n) asm volatile("s_waitcnt vmcnt(%0)" ::"n"(n) : "memory")
#define WAITL0() asm volatile("s_waitcnt lgkmcnt(0)" ::: "memory")
#define BAR() asm volatile("s_waitcnt lgkmcnt(0)\n\ts_barrier" ::: "memory")
#define WAITBAR(n) asm volatile("s_waitcnt vmcnt(%0) lgkmcnt(0)\n\ts_barrier" ::"n"(n) : "memory")

#define STAMP(i)
#define STAMP_DECL
#define STAMP_FLUSH()

#define NOSC1 0
template <int SC1>
DI void dma16(const void* base, u32 voff, u32 lds) {
    if constexpr (SC1 && !NOSC1) asm volatile("s_mov_b32 m0, %2\n\ts_nop 0\n\tglobal_load_lds_dwordx4 %0, %1 sc1" ::"v"(voff), "s"(base), "s"(lds) : "memory");
    else asm volatile("s_mov_b32 m0, %2\n\ts_nop 0\n\tglobal_load_lds_dwordx4 %0, %1" ::"v"(voff), "s"(base), "s"(lds) : "memory");
}
DI u32 ld32_sc1(const void* b, u32 o) { u32 v; asm volatile("global_load_dword %0, %1, %2 sc1" : "=v"(v) : "v"(o), "s"(b) : "memory"); return v; }
DI u32 ld16(const void* b, u32 o) { u32 v; asm volatile("global_load_ushort %0, %1, %2" : "=v"(v) : "v"(o), "s"(b) : "memory"); return v; }
DI u32 ld32(const void* b, u32 o) { u32 v; asm volatile("global_load_dword %0, %1, %2" : "=v"(v) : "v"(o), "s"(b) : "memory"); return v; }
DI u32x2 ld64_sc1(const void* b, u32 o) { u32x2 v; asm volatile("global_load_dwordx2 %0, %1, %2 sc1" : "=v"(v) : "v"(o), "s"(b) : "memory"); return v; }
DI u32x4 ld128_sc1(const void* b, u32 o) { u32x4 v; asm volatile("global_load_dwordx4 %0, %1, %2 sc1" : "=v"(v) : "v"(o), "s"(b) : "memory"); return v; }
DI void st32_sc1(void* b, u32 o, u32 v) { asm volatile("global_store_dword %0, %1, %2 sc1" ::"v"(o), "v"(v), "s"(b) : "memory"); }
DI void st64_sc1(void* b, u32 o, u32x2 v) { asm volatile("global_store_dwordx2 %0, %1, %2 sc1\n\ts_nop 1" ::"v"(o), "v"(v), "s"(b) : "memory"); }
DI void st128_sc1(void* b, u32 o, u32x4 v) { asm volatile("global_store_dwordx4 %0, %1, %2 sc1\n\ts_nop 1" ::"v"(o), "v"(v), "s"(b) : "memory"); }
template <int N>
DI u32 take32(u32 v) { u32 o; asm volatile("s_waitcnt vmcnt(%2)\n\tv_mov_b32 %0, %1" : "=v"(o) : "v"(v), "n"(N) : "memory"); return o; }

DI void poll32(const void* b, u32 o, u32 tag) {
#pragma unroll 1
    for (int it = 0; it < SPIN_CAP; ++it) {
        u32 v;
        asm volatile("global_load_dword %0, %1, %2 sc1\n\ts_waitcnt vmcnt(0)" : "=v"(v) : "v"(o), "s"(b) : "memory");
        if (wall(v == tag)) break;
        __builtin_amdgcn_s_sleep(4);
    }
}
DI void poll128(const void* b, u32 o, u32 tag) {
#pragma unroll 1
    for (int it = 0; it < SPIN_CAP; ++it) {
        u32x4 v;
        asm volatile("global_load_dwordx4 %0, %1, %2 sc1\n\ts_waitcnt vmcnt(0)" : "=v"(v) : "v"(o), "s"(b) : "memory");
        if (wall(v.x == tag && v.y == tag && v.z == tag && v.w == tag)) break;
        __builtin_amdgcn_s_sleep(4);
    }
}
DI bf16x8 trfrag(const char* a0, const char* a1) {
    s16x4 t0 = __builtin_amdgcn_ds_read_tr16_b64_v4i16((LDSP s16x4*)a0);
    s16x4 t1 = __builtin_amdgcn_ds_read_tr16_b64_v4i16((LDSP s16x4*)a1);
    return __builtin_bit_cast(bf16x8, __builtin_shufflevector(t0, t1, 0, 1, 2, 3, 4, 5, 6, 7));
}
DI f32x4 mfma(bf16x8 a, bf16x8 b, f32x4 c) { return __builtin_amdgcn_mfma_f32_16x16x32_bf16(a, b, c, 0, 0, 0); }
DI f32x4 mfmah(f16x8 a, f16x8 b, f32x4 c) { return __builtin_amdgcn_mfma_f32_16x16x32_f16(a, b, c, 0, 0, 0); }
DI f32x16 mfma32(bf16x8 a, bf16x8 b, f32x16 c) { return __builtin_amdgcn_mfma_f32_32x32x16_bf16(a, b, c, 0, 0, 0); }

struct Cplx { float r, i; };
DI Cplx cmul(Cplx a, Cplx b) { return {__builtin_fmaf(a.r, b.r, -(a.i * b.i)), __builtin_fmaf(a.r, b.i, a.i * b.r)}; }
DI Cplx csq(Cplx a) { return {__builtin_fmaf(a.r, a.r, -(a.i * a.i)), (a.r + a.r) * a.i}; }

constexpr int g1_ops(int n) { return n == 0 ? 0 : ((n + 3 <= 19 ? 1 : 0) + (((n & 1) == 0 && n / 2 + 2 <= 9) ? 2 : 0)); }
constexpr int g1_allow(int n) {
    if (n == 0) return 7;
    if (n == 1) return 6;
    if (n == 2) return 3 + g1_ops(1);
    if (n == 3) return 2 + g1_ops(1) + g1_ops(2);
    return (g1_ops(n - 3) - 1) + g1_ops(n - 2) + g1_ops(n - 1);
}
constexpr int g2_hi(int i) { int m = (10 * (i - 1) + 64) / 8; if (m < 8) m = 8; return m > 34 ? 34 : m; }
constexpr int g2_lo(int i) { return i == 0 ? 8 : g2_hi(i - 1); }
constexpr int g2_ops(int i, int AD) { return (g2_hi(i) - g2_lo(i)) + ((i + AD < 27) ? 1 : 0) + (i == 20 ? 2 : 0) + (i == 23 ? 1 : 0); }
constexpr int g2_allow(int I, int AD) {
    int a = 0;
    if (I < AD) { a = AD - 1 - I; for (int i = 0; i < I; ++i) a += g2_ops(i, AD); }
    else { a = g2_ops(I - AD, AD) - (g2_hi(I - AD) - g2_lo(I - AD)) - 1; for (int i = I - AD + 1; i < I; ++i) a += g2_ops(i, AD); }
    const int need = (10 * I + 9) / 8;
    if (need >= 8) {
        int it = 0;
        while (g2_hi(it) <= need) ++it;
        int b = g2_ops(it, AD) - (need - g2_lo(it)) - 1;
        for (int i = it + 1; i < I; ++i) b += g2_ops(i, AD);
        if (b < a) a = b;
    }
    return a;
}
#define SB() __builtin_amdgcn_sched_barrier(0)

__global__ __launch_bounds__(512, 4) void fused_kernel(
    const u16* __restrict__ x, const u16* __restrict__ Are, const u16* __restrict__ Aim, const u16* __restrict__ Bre,
    const u16* __restrict__ Bim, const u16* __restrict__ Cre, const u16* __restrict__ Cim, const u16* __restrict__ logdt,
    const u16* __restrict__ Dsk, const u16* __restrict__ w1, const u16* __restrict__ b1, const u16* __restrict__ w2,
    u16* __restrict__ out, char* __restrict__ ws, int mask) {
    extern __shared__ __attribute__((aligned(16))) char smem[];
    const int tid = __builtin_amdgcn_workitem_id_x();
    const int l = tid & 63;
    const int w = __builtin_amdgcn_readfirstlane(tid >> 6);
    const int g = l >> 4, c16 = l & 15;
    const int bid = __builtin_amdgcn_workgroup_id_x();
    const u32 lb = (u32)(size_t)(LDSP char*)smem;
    STAMP_DECL;
    STAMP(0);

    auto w2_load = [&](int ks, u32x4 (&wv)[5]) {
#pragma unroll
        for (int i = 0; i < 5; ++i) {
            const int n = i * 64 + l;
            wv[i] = *(const u32x4*)((const char*)w2 + (u32)((ks * 32 + n / 10) * 1280 + w * 160 + (n % 10) * 16));
        }
    };
    auto w2_store = [&](int ks, const u32x4 (&wv)[5]) {
        char* tile = smem + w * 5632;
#pragma unroll
        for (int i = 0; i < 5; ++i) {
            const int n = i * 64 + l;
            *(u32x4*)(tile + (n / 10) * 176 + (n % 10) * 16) = wv[i];
        }
#pragma unroll
        for (int dd = 0; dd < 5; ++dd) {
            const char* a = tile + (16 * (g >> 1) + 4 * (g & 1) + (c16 >> 2)) * 176 + dd * 32 + 8 * (l & 3);
            const bf16x8 f = trfrag(a, a + 8 * 176);
            st128_sc1(ws, WS_W2T + (u32)(((w * 54 + ks) * 5 + dd) * 1024 + l * 16), __builtin_bit_cast(u32x4, f));
        }
    };

    if (bid >= 256) {
        __builtin_amdgcn_s_setprio(2);
        const int gb = bid - 256;
        const int xcd = gb & 7, idx = gb >> 3;
        const int tq = w & 3, fh = w >> 2;
        const int t2 = 8 * (xcd >> 1) + (idx & 7), cg = 4 * (xcd & 1) + (idx >> 3);
        u32 tag;
        bool w2ready = false;
        const bool has1 = (idx < 28) && (mask & 4);
        const bool has2 = (mask & 8) != 0;
        f32x16 acc0, acc1;
        u32x2 st0;
        const int tt = 4 * (xcd >> 1) + (idx & 3);
        const int ct = 7 * (xcd & 1) + (idx >> 2);
        const int r32 = l & 31, h = l >> 5;
        auto warm = [&]() {
            const int n = idx * 512 + tid;
            const int m = n - 5120, row = m / 14, col = m - row * 14;
            const bool isx = n < 5120;
            const bool ok = isx || (row < 640 && (col < 13 || !(xcd & 1)));
            const u32 off = isx ? (u32)((xcd >> 1) * 655360 + n * 128) : (ok ? (u32)(row * 3456 + (xcd & 1) * 1792 + col * 128) : 0u);
            const char* base = (const char*)(isx ? x : w1);
            u32 v;
            asm volatile("global_load_dword %0, %1, off" : "=v"(v) : "v"(base + off) : "memory");
            return v;
        };
        u32 wrm = 0;
        if (has1) {
            const u32 ev = ld32_sc1(ws, WS_EPOCH);
            const bool half = (ct == 13);
            const u32 gx = (u32)((tt * 128 + 8 * w + (l >> 3)) * 1280 + (((l & 7) ^ ((4 * w + (l >> 4)) & 7)) << 4));
            int wch = (l & 15) ^ (((l >> 4) << 2) | (w & 3));
            if (half) wch &= 7;
            const u32 gw = (u32)((4 * w + (l >> 4)) * 3456 + ct * 256 + (wch << 4));
            const u32 ldst = lb + w * 1024;
            const int j0 = (3 * (idx >> 2) + ((5 * (idx & 3)) >> 1)) % 10;
            auto issueW = [&](auto NC) {
                constexpr int N = decltype(NC)::value, j = N >> 1;
                const int jr = (j + j0 >= 10) ? j + j0 - 10 : j + j0;
                dma16<0>(w1, gw + jr * (64 * 3456) + (N & 1) * (32 * 3456), ldst + (N & 3) * 8192);
            };
            auto issueX = [&](auto JC, auto UC) {
                constexpr int j = decltype(JC)::value, U = decltype(UC)::value;
                const int jr = (j + j0 >= 10) ? j + j0 - 10 : j + j0;
                dma16<0>(x, gx + U * (64 * 1280) + jr * 128, ldst + 32768 + (j % 3) * 16384 + U * 8192);
            };
            issueW(IC<0>{}); issueX(IC<0>{}, IC<0>{}); issueX(IC<0>{}, IC<1>{}); issueW(IC<1>{}); issueW(IC<2>{});
            issueX(IC<1>{}, IC<0>{}); issueX(IC<1>{}, IC<1>{}); issueW(IC<3>{}); issueX(IC<2>{}, IC<0>{}); issueX(IC<2>{}, IC<1>{});

            const int xoff = (r32 * 128 + ((h ^ ((r32 >> 1) & 7)) << 4) + (tq & 1) * 4096) ^ ((tq >> 1) << 5);
            const int xa = xoff + 32768 + (tq >> 1) * 8192;
            int wa[2];
            {
                const int q = c16 >> 2, p = l & 3, blk = (l >> 4) & 1;
#pragma unroll
                for (int t = 0; t < 2; ++t) {
                    const int row = 8 * h + 4 * t + q;
                    const int f = (q << 2) | ((2 * h + t) & 3);
                    wa[t] = (row * 256 + (((2 * blk + (p >> 1)) ^ f) << 4) + 8 * (p & 1)) ^ (fh << 7) ^ ((tq & 1) << 6) ^ ((tq >> 1) << 12);
                }
            }
#pragma unroll
            for (int i = 0; i < 16; ++i) { acc0[i] = 0.f; acc1[i] = 0.f; }
            float cs = 0.f;
            u32 w2flag = 0, bv = 0;
            const bf16x2 ones = __builtin_bit_cast(bf16x2, 0x3F803F80u);
            bf16x8 xf[2][2], wf[2][2][2];

            auto rdx = [&](auto NC, auto KC) {
                constexpr int N = decltype(NC)::value, K = decltype(KC)::value;
                xf[N & 1][K] = *(const bf16x8*)(smem + ((N >> 1) % 3) * 16384 + (xa ^ (((N & 1) << 6) | (K << 5))));
            };
            auto rdw = [&](auto NC, auto KC, auto CC) {
                constexpr int N = decltype(NC)::value, K = decltype(KC)::value, C = decltype(CC)::value;
                wf[N & 1][K][C] = trfrag(smem + (N & 3) * 8192 + (wa[0] ^ (C << 6) ^ (K << 12)), smem + (N & 3) * 8192 + (wa[1] ^ (C << 6) ^ (K << 12)));
            };
            auto step = [&](auto NC) {
                constexpr int N = decltype(NC)::value, B = (N & 1) ^ 1;
                constexpr int extra = (N == 19) ? 3 : ((N <= 3 && WARM) ? 1 : 0);
                constexpr bool dx = N >= 1 && (N & 1) == 0 && N / 2 + 2 <= 9;
                SB();
                if constexpr (N < 20) WAITBAR(g1_allow(N) + extra);
                else asm volatile("s_waitcnt vmcnt(0) lgkmcnt(0)\n\ts_barrier" : "+v"(st0), "+v"(w2flag), "+v"(bv), "+v"(wrm)::"memory");
                if constexpr (N >= 1) acc0 = mfma32(wf[B][0][0], xf[B][0], acc0);
                if constexpr (N < 20) { rdx(NC, IC<0>{}); rdw(NC, IC<0>{}, IC<0>{}); rdw(NC, IC<0>{}, IC<1>{}); }
                SB();
                if constexpr (N >= 1) acc1 = mfma32(wf[B][0][1], xf[B][0], acc1);
                if constexpr (N < 20) { rdx(NC, IC<1>{}); rdw(NC, IC<1>{}, IC<0>{}); rdw(NC, IC<1>{}, IC<1>{}); }
                SB();
                if constexpr (N >= 1) acc0 = mfma32(wf[B][1][0], xf[B][1], acc0);
                if constexpr (N >= 1 && N + 3 <= 19) issueW(IC<N + 3>{});
                SB();
                if constexpr (N >= 1) acc1 = mfma32(wf[B][1][1], xf[B][1], acc1);
                if constexpr (dx) { issueX(IC<N / 2 + 2>{}, IC<0>{}); issueX(IC<N / 2 + 2>{}, IC<1>{}); }
                if constexpr (N >= 1) {
                    const u32x4 wv = __builtin_bit_cast(u32x4, wf[B][0][0]);
#pragma unroll
                    for (int e = 0; e < 4; ++e) {
                        const u32 we = wv[e];
                        cs = __builtin_amdgcn_fdot2_f32_bf16(__builtin_bit_cast(bf16x2, we), ones, cs, false);
                    }
                }
                if constexpr (N == 0) {
                    STAMP(1);
                    tag = mktag(take32<63>(ev));
                    if (tid == 0) st32_sc1(ws, WS_RD + bid * 4, tag);
                }
                if constexpr (N == 18) {
                    st0 = ld64_sc1(ws, WS_STAT + (u32)((tt * 128 + 32 * tq + r32) * 8));
                    w2flag = ld32_sc1(ws, WS_W2F + (u32)(cg * 256 + (l < 54 ? l : 0) * 4));
                    bv = ld16(b1, (u32)((ct * 128 + (tid & (half ? 63 : 127))) * 2));
                }
            };
            step(IC<0>{}); step(IC<1>{}); step(IC<2>{}); step(IC<3>{}); step(IC<4>{}); step(IC<5>{}); step(IC<6>{});
            step(IC<7>{}); step(IC<8>{}); step(IC<9>{}); step(IC<10>{}); step(IC<11>{}); step(IC<12>{}); step(IC<13>{});
            step(IC<14>{}); step(IC<15>{}); step(IC<16>{}); step(IC<17>{}); step(IC<18>{}); step(IC<19>{});
            step(IC<20>{});
            w2ready = wall(w2flag == tag);
            STAMP(2);
            cs += asf((u32)__builtin_amdgcn_ds_bpermute((l ^ 32) * 4, (int)asu(cs)));
            float* csum = (float*)(smem + 73728);
            if (l < 32) csum[(tq >> 1) * 128 + fh * 64 + (tq & 1) * 32 + l] = cs;
            if (tid < 128) csum[256 + tid] = bf2f(bv) * 1.44269504f;
        } else {
            const u32 ev = ld32_sc1(ws, WS_EPOCH);
            const bool dw = idx >= 28 && (mask & 1);
            const int k0 = gb - 224;
            const bool dw2 = dw && k0 < 22;
            u32x4 wv[5], wv2[5];
            if (dw) w2_load(k0, wv);
            if (dw2) w2_load(k0 + 32, wv2);
            tag = mktag(take32<0>(ev));
            asm volatile("" : "+v"(wrm));
            if (dw) w2_store(k0, wv);
            if (dw2) w2_store(k0 + 32, wv2);
            WAITBAR(0);
            if (dw && l == 0) st32_sc1(ws, WS_W2F + (u32)((w * 64 + k0) * 4), tag);
            if (dw2 && l == 0) st32_sc1(ws, WS_W2F + (u32)((w * 64 + k0 + 32) * 4), tag);
            if (tid == 0) st32_sc1(ws, WS_RD + bid * 4, tag);
        }

        const u32 gw2 = WS_W2T + (u32)(cg * 54 * 5120 + l * 16);
        const int i0 = ((((idx & 7) * 27) >> 3) + (idx >> 3) * 7) % 27;
        auto rot = [&](int I) { return (I + i0 >= 27) ? I + i0 - 27 : I + i0; };
        int fsrc = 10 * i0 + w;
        auto issue_w2 = [&](auto MC) {
            constexpr int M = decltype(MC)::value;
            if (M < 33 || w < 6) dma16<1>(ws, gw2 + fsrc * 1024, lb + (M & 7) * 8192 + w * 1024);
            else dma16<1>(ws, gw2, lb + 73728 + 2048 + (w & 1) * 1024);
            fsrc += 8;
            if (fsrc >= 270) fsrc -= 270;
        };
        auto w2_prefetch = [&]() {
            issue_w2(IC<0>{}); issue_w2(IC<1>{}); issue_w2(IC<2>{}); issue_w2(IC<3>{});
            issue_w2(IC<4>{}); issue_w2(IC<5>{}); issue_w2(IC<6>{}); issue_w2(IC<7>{});
        };
        if (has2 && w2ready) w2_prefetch();

        if (has1) {
            const bool active = !(ct == 13 && fh == 1);
            BAR();
            if (active) {
#pragma unroll 1
                for (int it = 0; it < SPIN_CAP; ++it) {
                    if (wall(st0.y == tag)) break;
                    asm volatile("global_load_dwordx2 %0, %1, %2 sc1\n\ts_waitcnt vmcnt(0)" : "=v"(st0)
                                 : "v"(WS_STAT + (u32)((tt * 128 + 32 * tq + r32) * 8)), "s"(ws) : "memory");
                }
                STAMP(3);
                const float nmt = -lo16(st0.x), rt = 1.44269504f * __builtin_amdgcn_rcpf(hi16(st0.x));
                const float* csum = (const float*)(smem + 73728) + fh * 64 + 4 * h;
                const u32 abase = WS_ACT + (u32)(((tt * 8 + 2 * tq + (r32 >> 4)) * 54 + ct * 4 + fh * 2) * 1024 + (h * 16 + c16) * 16);
                auto epi = [&](const f32x16& a, int c) {
#pragma unroll
                    for (int s = 0; s < 2; ++s) {
                        u32 pk[4];
#pragma unroll
                        for (int mm = 0; mm < 2; ++mm) {
                            const int m = 2 * s + mm;
                            const f32x4 ca = *(const f32x4*)(csum + c * 32 + 8 * m), cb4 = *(const f32x4*)(csum + 128 + c * 32 + 8 * m);
                            const f32x4 b4 = *(const f32x4*)(csum + 256 + c * 32 + 8 * m);
                            float r[4];
#pragma unroll
                            for (int i = 0; i < 4; ++i) {
                                const float t1 = __builtin_fmaf(nmt, cb4[i], __builtin_fmaf(nmt, ca[i], a[4 * m + i]));
                                const float h2 = __builtin_fmaf(t1, rt, b4[i]);
                                const float e = __builtin_amdgcn_exp2f(h2);
                                const float rr = __builtin_amdgcn_rcpf(__builtin_fmaf(e, e + 2.f, 2.f));
                                r[i] = h2 * __builtin_fmaf(-1.38629436f, rr, 0.69314718f);
                            }
                            pk[2 * mm] = pk2(r[0], r[1]);
                            pk[2 * mm + 1] = pk2(r[2], r[3]);
                        }
                        st128_sc1(ws, abase + c * 1024 + s * 512, u32x4{pk[0], pk[1], pk[2], pk[3]});
                    }
                };
                epi(acc0, tq & 1);
                epi(acc1, (tq & 1) ^ 1);
                STAMP(4);
                WAITV(0);
                STAMP(5);
                if (l == 0) st32_sc1(ws, WS_ACTF + (u32)(((tt * 4 + tq) * 32 + ct * 2 + fh) * 4), tag);
            }
        }

        if (has2) {
            const int tbk = w & 3, kp = w >> 2;
            if (!w2ready) {
                poll32(ws, WS_W2F + (u32)(cg * 256 + (l < 54 ? l : 0) * 4), tag);
                w2_prefetch();
            }
            if (w == 0) poll32(ws, WS_ACTF + (u32)((t2 * 64 + (r32 < 27 ? l : 0)) * 4), tag);
            BAR();
            STAMP(6);
            const u32 ga = WS_ACT + (u32)(((4 * t2 + tbk) * 54 + kp) * 1024 + l * 16);
            constexpr int AD = 7;
            u32x4 af[8];
#pragma unroll
            for (int i = 0; i < AD; ++i) af[i] = ld128_sc1(ws, ga + rot(i) * 2048);
            f32x4 a2[5];
#pragma unroll
            for (int d = 0; d < 5; ++d) a2[d] = f32x4{0, 0, 0, 0};
            const int wl = kp * 5120 + l * 16;
            u32 sf0 = 0, sf1 = 0;
            bool sdone = false;
            const int n0 = w * 64 + l, n1 = 512 + (w & 1) * 64 + l;
            const u32 gx0 = (u32)((t2 * 64 + n0 / 10) * 1280 + cg * 160 + (n0 % 10) * 16);
            const u32 gx1 = (u32)((t2 * 64 + n1 / 10) * 1280 + cg * 160 + (n1 % 10) * 16);
            const u32 gs0 = WS_ST + (u32)((cg * 80 + (n0 >> 3)) * 4096 + t2 * 128 + (n0 & 7) * 16);
            const u32 gs1 = WS_ST + (u32)((cg * 80 + (n1 >> 3)) * 4096 + t2 * 128 + (n1 & 7) * 16);
            constexpr int XS = 16384, SS = 65536;
            const u32 sfp = WS_SF + (u32)(((t2 >> 4) * 640 + cg * 80) * 4);
            bf16x8 wq[2][5];
            auto load_s = [&]() {
                dma16<1>(ws, gs0, lb + SS + w * 1024);
                if (w < 2) dma16<1>(ws, gs1, lb + SS + 8192 + w * 1024);
            };
            auto st2 = [&](auto IC_) {
                constexpr int I = decltype(IC_)::value, B = I & 1;
                constexpr int allow = g2_allow(I, AD);
                SB();
                if constexpr (I == 24)
                    asm volatile("s_waitcnt vmcnt(%3)\n\ts_barrier" : "+v"(af[I & 7]), "+v"(sf0), "+v"(sf1) : "n"(allow < g2_ops(21, AD) + g2_ops(22, AD) + g2_ops(23, AD) ? allow : g2_ops(21, AD) + g2_ops(22, AD) + g2_ops(23, AD)) : "memory");
                else
                    asm volatile("s_waitcnt vmcnt(%1)\n\ts_barrier" : "+v"(af[I & 7]) : "n"(allow) : "memory");
                const bf16x8 bfrag = __builtin_bit_cast(bf16x8, af[(I + 7) & 7]);
#pragma unroll
                for (int d = 0; d < 5; ++d) {
                    if constexpr (I >= 1) a2[d] = mfma(wq[B ^ 1][d], bfrag, a2[d]);
                    {
                        const int o0 = ((10 * I + d) & 63) * 1024;
                        if (o0 + 5120 + 1024 <= 65536) wq[B][d] = *(const bf16x8*)(smem + wl + o0);
                        else wq[B][d] = *(const bf16x8*)(smem + ((wl + o0) & 65535));
                    }
                    SB();
                }
                if constexpr (g2_lo(I) < g2_hi(I)) issue_w2(IC<g2_lo(I)>{});
                if constexpr (g2_lo(I) + 1 < g2_hi(I)) issue_w2(IC<g2_lo(I) + 1>{});
                if constexpr (I + AD < 27) af[(I + AD) & 7] = ld128_sc1(ws, ga + rot(I + AD) * 2048);
                if constexpr (I == 20) {
                    sf0 = ld32_sc1(ws, sfp + l * 4);
                    sf1 = ld32_sc1(ws, sfp + 256 + c16 * 4);
                }
                if constexpr (I == 23) {
                    dma16<0>(x, gx0, lb + XS + w * 1024);
                    if (w < 2) dma16<0>(x, gx1, lb + XS + 8192 + w * 1024);
                }
                if constexpr (I == 24) {
                    sdone = wall(sf0 == tag && sf1 == tag);
                    if (sdone) load_s();
                }
            };
            st2(IC<0>{}); st2(IC<1>{}); st2(IC<2>{}); st2(IC<3>{}); st2(IC<4>{}); st2(IC<5>{}); st2(IC<6>{}); st2(IC<7>{}); st2(IC<8>{});
            st2(IC<9>{}); st2(IC<10>{}); st2(IC<11>{}); st2(IC<12>{}); st2(IC<13>{}); st2(IC<14>{}); st2(IC<15>{}); st2(IC<16>{});
            st2(IC<17>{}); st2(IC<18>{}); st2(IC<19>{}); st2(IC<20>{}); st2(IC<21>{}); st2(IC<22>{}); st2(IC<23>{}); st2(IC<24>{});
            st2(IC<25>{}); st2(IC<26>{});
            {
                const bf16x8 bfrag = __builtin_bit_cast(bf16x8, af[26 & 7]);
#pragma unroll
                for (int d = 0; d < 5; ++d) a2[d] = mfma(wq[0][d], bfrag, a2[d]);
            }
            STAMP(7);
            if (!sdone) {
                poll32(ws, sfp + l * 4, tag);
                poll32(ws, sfp + 256 + c16 * 4, tag);
                load_s();
            }
            char* ex = smem + 30720 + tbk * 5120 + l * 16;
            if (kp == 0) { *(f32x4*)(ex + 3072) = a2[3]; *(f32x4*)(ex + 4096) = a2[4]; }
            else { *(f32x4*)(ex) = a2[0]; *(f32x4*)(ex + 1024) = a2[1]; *(f32x4*)(ex + 2048) = a2[2]; }
            WAITBAR(0);
            STAMP(8);
            auto fin = [&](int d, f32x4 o) {
                o += *(const f32x4*)(ex + d * 1024);
                char* xp = smem + XS + (16 * tbk + c16) * 160 + d * 32 + 8 * g;
                const u32x2 xv = *(const u32x2*)xp;
                const s16x4 sv4 = __builtin_amdgcn_ds_read_tr16_b64_v4i16(
                    (LDSP s16x4*)(smem + SS + (16 * d + 4 * g + (c16 >> 2)) * 128 + tbk * 32 + 8 * (l & 3)));
                const u32x2 sv = __builtin_bit_cast(u32x2, sv4);
                const float xs[4] = {lo16(xv.x), hi16(xv.x), lo16(xv.y), hi16(xv.y)};
                const float ss[4] = {lo16(sv.x), hi16(sv.x), lo16(sv.y), hi16(sv.y)};
                float r[4];
#pragma unroll
                for (int i = 0; i < 4; ++i) {
                    const float sg = __builtin_amdgcn_rcpf(1.f + __builtin_amdgcn_exp2f(-1.44269504f * o[i]));
                    r[i] = __builtin_fmaf(ss[i] * o[i], sg, xs[i]);
                }
                *(u32x2*)xp = u32x2{pk2(r[0], r[1]), pk2(r[2], r[3])};
            };
            if (kp == 0) { fin(0, a2[0]); fin(1, a2[1]); fin(2, a2[2]); }
            else { fin(3, a2[3]); fin(4, a2[4]); }
            BAR();
            *(u32x4*)((char*)out + gx0) = *(const u32x4*)(smem + XS + n0 * 16);
            if (w < 2) *(u32x4*)((char*)out + gx1) = *(const u32x4*)(smem + XS + n1 * 16);
        }
        STAMP(9);
        if (mask & 12) STAMP_FLUSH();
        return;
    }

    const int cb = bid;
    const int cx = cb & 7, ci = cb >> 3;
    const int tg = 64 * (cx >> 1) + 32 * (cx & 1) + ci;
    const int tk = w >> 2;
    const int ch = (w >> 1) & 1, p = w & 1;
    const bool doconv = (mask & 2) && (tk == 0 || ci >= 24);
    const int d0 = 80 * cx + 2 * (tk == 0 ? ci : ci + 8);
    const int d = d0 + ch;
    u32 xv[5];
    if (mask & 1) {
        const u32* xr = (const u32*)x + (u32)((tg * 8 + w) * 320 + l);
#pragma unroll
        for (int i = 0; i < 5; ++i) xv[i] = xr[64 * i];
    }
    const u32 ev = ld32_sc1(ws, WS_EPOCH);
    u32 pa[6] = {0, 0, 0, 0, 0, 0}, pld = 0, pdk = 0;
    if (doconv) {
        const u32 pi = (u32)(d * NM + p * 64 + l);
        pa[0] = Are[pi]; pa[1] = Aim[pi]; pa[2] = Bre[pi]; pa[3] = Bim[pi]; pa[4] = Cre[pi]; pa[5] = Cim[pi];
        pld = logdt[d]; pdk = Dsk[d];
    }
    char* lnt = smem + 65536;
    float mb = 0.f, den = 1.f;
    if (mask & 1) {
        float s = 0.f;
#pragma unroll
        for (int i = 0; i < 5; ++i) s += lo16(xv[i]) + hi16(xv[i]);
        const float mean = wave_sum(s) * (1.f / 640.f);
        float q = 0.f;
#pragma unroll
        for (int i = 0; i < 5; ++i) {
            const float a = lo16(xv[i]) - mean, b = hi16(xv[i]) - mean;
            q = __builtin_fmaf(a, a, q); q = __builtin_fmaf(b, b, q);
        }
        const float var = wave_sum(q) * (1.f / 640.f);
        mb = rbf(mean);
        den = rbf(__builtin_sqrtf(rbf(rbf(var) + 1e-5f)));
        const float rden = 1.0f / den;
        char* lw = lnt + w * 1280 + l * 4 + w * 64;
#pragma unroll
        for (int i = 0; i < 5; ++i) {
            const u32 dd = pk2(lo16(xv[i]) - mb, hi16(xv[i]) - mb);
            const u32 o = pk2(lo16(dd) * rden, hi16(dd) * rden);
            if (i < 3) *(u32*)(lw + 256 * i) = o;
            else *(u32*)(lw + 256 * i - ((256 * i + l * 4 + w * 64 >= 1280) ? 1280 : 0)) = o;
        }
    }
    const u32 tag = mktag(doconv ? take32<8>(ev) : take32<0>(ev));
    STAMP(1);
    if ((mask & 1) && l == 0) st64_sc1(ws, WS_STAT + (u32)((tg * 8 + w) * 8), u32x2{pk2(mb, den), tag});
    BAR();
    if (tid == 0) st32_sc1(ws, WS_RD + bid * 4, tag);
    if (mask & 1) {
        const int q = c16 >> 2;
        auto piece = [&](int i) {
            int p0 = 2 * (i * 4 + g) + 4 * q, p1 = p0 + 16;
            if (p0 >= 80) p0 -= 80;
            if (p1 >= 80) p1 -= 80;
            const bf16x8 f = trfrag(lnt + q * 1280 + p0 * 16 + 8 * (l & 3), lnt + (q + 4) * 1280 + p1 * 16 + 8 * (l & 3));
            st128_sc1(ws, WS_XB + (u32)(tg * 10240 + i * 1024 + l * 16), __builtin_bit_cast(u32x4, f));
        };
        piece(w);
        if (w >= 6) piece(2 + w);
    }
    STAMP(2);
    if (!doconv) {
        WAITBAR(0);
        if (!(mask & 2) && (mask & 1) && tid == 0) st32_sc1(ws, WS_XBF + cb * 4, tag);
        if (cb == 0 && w == 7) {
            poll128(ws, WS_RD + l * 16, tag);
            poll128(ws, WS_RD + 1024 + l * 16, tag);
            if (l == 0) st32_sc1(ws, WS_EPOCH, tag);
        }
        return;
    }
    {
        const float dt = rbf(__builtin_expf(bf2f(pld)));
        const float lr2 = (dt * bf2f(pa[0])) * 1.44269504f;
        const float li = dt * bf2f(pa[1]);
        const float rh = li * 0.15915494f;
        const float rl = __builtin_fmaf(li, 0.15915494f, -rh) + li * 3.0908620e-9f;
        auto E = [&](float t) {
            const float ph = rh * t;
            const float er = __builtin_fmaf(rl, t, __builtin_fmaf(rh, t, -ph));
            const float fr = (ph - __builtin_rintf(ph)) + er;
            const float m = __builtin_amdgcn_exp2f(lr2 * t);
            return Cplx{m * __builtin_amdgcn_cosf(fr), m * __builtin_amdgcn_sinf(fr)};
        };
        char* scr = smem + w * 4608;
        auto put = [&](int row, Cplx v) { *(u32*)(scr + row * 288 + l * 4) = pkh(v.r, v.i); };
        const char* frd = scr + c16 * 288 + g * 16;
        f16x8 bq[4];
        float inv_scale;
        {
            Cplx c = cmul(Cplx{bf2f(pa[4]), bf2f(pa[5])}, Cplx{dt * bf2f(pa[2]), dt * bf2f(pa[3])});
            const float mx = wave_max(__builtin_fmaxf(__builtin_fabsf(c.r), __builtin_fabsf(c.i)));
            int ex = 14 - __builtin_amdgcn_frexp_expf(mx);
            ex = ex > 100 ? 100 : (ex < -100 ? -100 : ex);
            inv_scale = __builtin_amdgcn_ldexpf(1.f, -ex);
            c.r = __builtin_amdgcn_ldexpf(c.r, ex);
            c.i = -__builtin_amdgcn_ldexpf(c.i, ex);
            Cplx e8 = E(8.f);
            e8.i = -e8.i;
            const Cplx e16 = csq(e8), e32 = csq(e16), e64 = csq(e32);
            Cplx b[8];
            b[0] = c; b[1] = cmul(c, e8); b[2] = cmul(c, e16); b[3] = cmul(b[1], e16);
#pragma unroll
            for (int j = 0; j < 4; ++j) b[4 + j] = cmul(b[j], e32);
#pragma unroll
            for (int j = 0; j < 8; ++j) { put(j, b[j]); put(8 + j, cmul(b[j], e64)); }
#pragma unroll
            for (int s = 0; s < 4; ++s) bq[s] = *(const f16x8*)(frd + s * 64);
        }
        WAITBAR(0);
        if (tid == 0 && (mask & 1)) st32_sc1(ws, WS_XBF + cb * 4, tag);
        u32 ix[9];
#pragma unroll
        for (int k = 0; k < 9; ++k) ix[k] = g_idx.v[k * 128 + p * 64 + l];
        auto tile_mm = [&]() {
            f32x4 c = {0, 0, 0, 0};
#pragma unroll
            for (int s = 0; s < 4; ++s) c = mfmah(*(const f16x8*)(frd + s * 64), bq[s], c);
            return c;
        };
        f32x4 T0, T1, T2;
        {
            const Cplx e1 = E(1.f), e2 = csq(e1), e3 = cmul(e2, e1), e4 = csq(e2), e5 = cmul(e4, e1), e6 = cmul(e4, e2), e7 = cmul(e4, e3);
            const Cplx e128 = E(128.f);
            put(0, Cplx{1.f, 0.f}); put(1, e1); put(2, e2); put(3, e3); put(4, e4); put(5, e5); put(6, e6); put(7, e7);
            put(8, e128); put(9, cmul(e128, e1)); put(10, cmul(e128, e2)); put(11, cmul(e128, e3));
            put(12, cmul(e128, e4)); put(13, cmul(e128, e5)); put(14, cmul(e128, e6)); put(15, cmul(e128, e7));
            T0 = tile_mm();
            const Cplx e256 = csq(e128), e384 = cmul(e256, e128), e512 = csq(e256);
            const Cplx e640 = cmul(e512, e128), e768 = cmul(e512, e256), e896 = cmul(e512, e384);
            put(0, e256); put(1, cmul(e256, e2)); put(2, cmul(e256, e4)); put(3, cmul(e256, e6));
            put(4, e384); put(5, cmul(e384, e2)); put(6, cmul(e384, e4)); put(7, cmul(e384, e6));
            put(8, e512); put(9, cmul(e512, e4)); put(10, e640); put(11, cmul(e640, e4));
            put(12, e768); put(13, cmul(e768, e4)); put(14, e896); put(15, cmul(e896, e4));
            T1 = tile_mm();
            const Cplx e1k = E(1024.f);
            put(0, e1k); put(1, cmul(e1k, e128)); put(2, cmul(e1k, e256)); put(3, cmul(e1k, e384)); put(4, cmul(e1k, e512));
            put(5, cmul(e1k, e640)); put(6, cmul(e1k, e768)); put(7, cmul(e1k, e896)); put(8, csq(e1k));
            T2 = tile_mm();
        }
        STAMP(3);
        {
            float* tabp = (float*)scr;
            const int j = c16;
            *(f32x4*)(tabp + 128 * (g >> 1) + 4 * (g & 1) + 8 * j) = T0 * inv_scale;
            T1 *= inv_scale;
            T2 *= inv_scale;
            if (g < 2) {
                *(f32x4*)(tabp + 256 + 64 * g + 4 * j) = T1;
#pragma unroll
                for (int i = 0; i < 4; ++i) tabp[512 + 16 * (4 * g + i) + j] = T2[i];
            } else {
                *(f32x2*)(tabp + 384 + 64 * (g - 2) + 2 * j) = f32x2{T1[0], T1[1]};
                *(f32x2*)(tabp + 384 + 64 * (g - 2) + 32 + 2 * j) = f32x2{T1[2], T1[3]};
                if (g == 2) tabp[640 + j] = T2[0];
            }
        }
        if (w == 0) poll128(ws, WS_XBF + l * 16, tag);
        BAR();
        STAMP(4);
        char* U = smem + 36864 + tk * 9216;
#pragma unroll
        for (int i = 0; i < 2; ++i) {
            const int c = ((w & 3) * 2 + i) * 32 + (l >> 1);
            const int chs = (l & 1) ^ ((c >> 3) & 1);
            dma16<1>(ws, WS_XB + (u32)(c * 10240 + (d0 + chs) * 16), lb + 36864 + tk * 9216 + 1024 + ((w & 3) * 2 + i) * 1024);
        }
        if ((w & 3) == 0) *(u32x4*)(U + l * 16) = u32x4{0, 0, 0, 0};
        char* tab = smem + 76288 + (w >> 1) * 1344;
        {
            const float* t0 = (const float*)(smem + (w & 6) * 4608);
            const float* t1 = (const float*)(smem + (w | 1) * 4608);
#pragma unroll
            for (int it = 0; it < 3; ++it) {
                const int i = it * 128 + p * 64 + l;
                if (i < 328) {
                    const f32x2 a = *(const f32x2*)(t0 + 2 * i), b = *(const f32x2*)(t1 + 2 * i);
                    *(u32*)(tab + 4 * i) = pk2(a.x + b.x, a.y + b.y);
                } else if (i == 330) *(u32*)(tab + 4 * i) = 0u;
            }
        }
        BAR();
        STAMP(5);
        char* KE = smem + (w >> 1) * 8448;
        char* KO = KE + 4160;
        {
            const int m = p * 64 + l;
            u32 e[9];
#pragma unroll
            for (int k = 0; k < 9; ++k) {
                u16x2 v;
                v.x = *(const u16*)(tab + (ix[k] & 0xFFFFu));
                v.y = *(const u16*)(tab + (ix[k] >> 16));
                e[k] = __builtin_bit_cast(u32, v);
            }
            *(u32x4*)(KE + m * 32) = u32x4{e[0], e[1], e[2], e[3]};
            *(u32x4*)(KE + m * 32 + 16) = u32x4{e[4], e[5], e[6], e[7]};
            u32 o[8];
#pragma unroll
            for (int k = 0; k < 8; ++k) o[k] = __builtin_amdgcn_alignbit(e[k + 1], e[k], 16);
            *(u32x4*)(KO + m * 32) = u32x4{o[0], o[1], o[2], o[3]};
            *(u32x4*)(KO + m * 32 + 16) = u32x4{o[4], o[5], o[6], o[7]};
            if (p == 0 && l < 4) *(u32x4*)(KE + 4096 + (l & 1) * 16 + (l >> 1) * 4160) = u32x4{0, 0, 0, 0};
        }
        WAITBAR(0);
        STAMP(6);
        f32x4 y[8];
#pragma unroll
        for (int a = 0; a < 8; ++a) y[a] = f32x4{0, 0, 0, 0};
#pragma unroll 1
        for (int rr = 0; rr < 4; ++rr) {
            const int r = 8 * p + 2 * rr;
            const int c = 2 * (c16 - r - 1) + g + 32;
            const char* ub = U + c * 32 + ((ch ^ ((c >> 3) & 1)) << 4);
            bf16x8 B[8];
#pragma unroll
            for (int a = 0; a < 8; ++a) B[a] = *(const bf16x8*)(ub + a * 1024);
            const int i0 = 2031 - 16 * r - c16 + 8 * g;
            const char* kb = ((i0 & 1) ? KO : KE) + (i0 >> 1) * 4;
#pragma unroll
            for (int q = 0; q < 8; ++q) {
                const u32x4a av = *(const u32x4a*)(kb - q * 512);
                const bf16x8 A = __builtin_bit_cast(bf16x8, av);
#pragma unroll
                for (int a = q; a < 8; ++a) y[a] = mfma(A, B[a - q], y[a]);
            }
        }
        STAMP(7);
        const float dsk = bf2f(pdk);
        BAR();
        char* ew = smem + w * 4096 + l * 16;
        const char* eo = smem + (w ^ 1) * 4096 + l * 16;
        char* so = smem + 55296 + w * 2048;
        auto fin = [&](int a, f32x4 yy) {
            yy += *(const f32x4*)(eo + a * 1024);
            const int cu = 2 * (16 * (4 * p + a) + c16) + (g >> 1) + 32;
            const u32x2 uv = *(const u32x2*)(U + cu * 32 + ((ch ^ ((cu >> 3) & 1)) << 4) + (g & 1) * 8);
            *(u32x2*)(so + a * 512 + c16 * 32 + g * 8) =
                u32x2{pk2(__builtin_fmaf(lo16(uv.x), dsk, yy[0]), __builtin_fmaf(hi16(uv.x), dsk, yy[1])),
                      pk2(__builtin_fmaf(lo16(uv.y), dsk, yy[2]), __builtin_fmaf(hi16(uv.y), dsk, yy[3]))};
        };
        if (p == 0) {
#pragma unroll
            for (int a = 0; a < 4; ++a) *(f32x4*)(ew + a * 1024) = y[4 + a];
            BAR();
#pragma unroll
            for (int a = 0; a < 4; ++a) fin(a, y[a]);
        } else {
#pragma unroll
            for (int a = 0; a < 4; ++a) *(f32x4*)(ew + a * 1024) = y[a];
            BAR();
#pragma unroll
            for (int a = 0; a < 4; ++a) fin(a, y[4 + a]);
        }
#pragma unroll
        for (int i = 0; i < 2; ++i) {
            const u32x4 v = *(const u32x4*)(so + i * 1024 + l * 16);
            st128_sc1(ws, WS_ST + (u32)(d * 4096 + p * 2048 + i * 1024 + l * 16), v);
        }
        STAMP(8);
        WAITV(0);
        if (l == 0) st32_sc1(ws, WS_SF + (u32)((p * 640 + d) * 4), tag);
        STAMP(9);
        STAMP_FLUSH();
    }
}
#if defined(__HIP_DEVICE_COMPILE__)
#pragma clang attribute pop
#endif

extern "C" void kernel_launch(void* const* d_in, const int* in_sizes, int n_in, void* d_out, int out_size, void* d_ws,
                              size_t ws_size, hipStream_t stream) {
    (void)hipFuncSetAttribute((const void*)fused_kernel, hipFuncAttributeMaxDynamicSharedMemorySize, LDS_BYTES);
#define A(i) (const u16*)d_in[i]
    fused_kernel<<<512, 512, LDS_BYTES, stream>>>(A(0), A(1), A(2), A(3), A(4), A(5), A(6), A(7), A(8), A(10), A(11), A(12),
                                                  (u16*)d_out, (char*)d_ws, 15);
}
```

```cpp
#include <hip/hip_runtime.h>
#include <stdint.h>
#include <type_traits>
#include <initializer_list>

typedef unsigned short u16;
typedef unsigned int u32;
typedef unsigned long long u64;
typedef __attribute__((ext_vector_type(8))) __bf16 bf16x8;
typedef __attribute__((ext_vector_type(2))) __bf16 bf16x2;
typedef __attribute__((ext_vector_type(8))) _Float16 f16x8;
typedef __attribute__((ext_vector_type(2))) _Float16 f16x2;
typedef __attribute__((ext_vector_type(4))) short s16x4;
typedef __attribute__((ext_vector_type(2))) u16 u16x2;
typedef __attribute__((ext_vector_type(4))) float f32x4;
typedef __attribute__((ext_vector_type(16))) float f32x16;
typedef __attribute__((ext_vector_type(2))) float f32x2;
typedef __attribute__((ext_vector_type(4))) u32 u32x4;
typedef __attribute__((ext_vector_type(2))) u32 u32x2;
typedef __attribute__((ext_vector_type(4), aligned(4))) u32 u32x4a;
#define LDSP __attribute__((address_space(3)))
#define DI __device__ __forceinline__
template <int N> using IC = std::integral_constant<int, N>;

#define MULTI 0

#define WARM 0
#define TLBP 0
constexpr int NM = 128;
constexpr u32 WS_EPOCH = 0, WS_RD = 1024, WS_XBF = 4096, WS_W2F = 5120, WS_SF = 8192, WS_ACTF = 16384, WS_STAT = 32768;
constexpr u32 WS_XB = 1u << 20, WS_ST = 4u << 20, WS_W2T = 8u << 20, WS_ACT = 12u << 20;
constexpr int LDS_BYTES = 81920;
constexpr int SPIN_CAP = 1 << 16;

struct IdxTab {
    u32 v[9 * 128];
    static constexpr int idx(int l) {
        if (l < 0) return 660;
        if (l < 256) return l;
        int sh = l < 512 ? 1 : (l < 1024 ? 2 : 3);
        int q = (l + ((1 << (sh - 1)) - 1) + ((l >> sh) & 1)) >> sh;
        return 128 * sh + q;
    }
    constexpr IdxTab() : v() {
        for (int k = 0; k < 9; ++k)
            for (int m = 0; m < 128; ++m)
                v[k * 128 + m] = (u32)(2 * idx(2047 - (16 * m + 2 * k))) | ((u32)(2 * idx(2047 - (16 * m + 2 * k + 1))) << 16);
    }
};
__device__ const IdxTab g_idx = IdxTab();

#if defined(__HIP_DEVICE_COMPILE__)
#pragma clang attribute push(__attribute__((target("no-packed-fp32-ops"))), apply_to = function)
#endif
DI float asf(u32 v) { return __builtin_bit_cast(float, v); }
DI u32 asu(float v) { return __builtin_bit_cast(u32, v); }
DI bool wall(bool p) { return __builtin_amdgcn_ballot_w64(p) == ~0ull; }
DI float bf2f(u32 v) { return asf(v << 16); }
DI u32 pk2(float a, float b) { f32x2 f = {a, b}; return __builtin_bit_cast(u32, __builtin_convertvector(f, bf16x2)); }
DI u32 pkh(float a, float b) { f32x2 f = {a, b}; return __builtin_bit_cast(u32, __builtin_convertvector(f, f16x2)); }
DI float rbf(float a) { return asf(pk2(a, a) << 16); }
DI float lo16(u32 v) { return asf(v << 16); }
DI float hi16(u32 v) { return asf(v & 0xFFFF0000u); }
DI u32 mktag(u32 e) { return e * 0x9E3779B1u + 0x7F4A7C15u; }
template <int CTRL, int RM>
DI float dpp(float v) { return asf((u32)__builtin_amdgcn_update_dpp(0, (int)asu(v), CTRL, RM, 0xf, true)); }
DI float wave_sum(float v) {
    v += dpp<0x111, 0xf>(v); v += dpp<0x112, 0xf>(v); v += dpp<0x114, 0xf>(v); v += dpp<0x118, 0xf>(v);
    v += dpp<0x142, 0xa>(v); v += dpp<0x143, 0xc>(v);
    return asf((u32)__builtin_amdgcn_readlane((int)asu(v), 63));
}
DI float wave_max(float v) {
    v = __builtin_fmaxf(v, dpp<0x111, 0xf>(v)); v = __builtin_fmaxf(v, dpp<0x112, 0xf>(v));
    v = __builtin_fmaxf(v, dpp<0x114, 0xf>(v)); v = __builtin_fmaxf(v, dpp<0x118, 0xf>(v));
    v = __builtin_fmaxf(v, dpp<0x142, 0xa>(v)); v = __builtin_fmaxf(v, dpp<0x143, 0xc>(v));
    return asf((u32)__builtin_amdgcn_readlane((int)asu(v), 63));
}

#define WAITV(n) asm volatile("s_waitcnt vmcnt(%0)" ::"n"(n) : "memory")
#define WAITL0() asm volatile("s_waitcnt lgkmcnt(0)" ::: "memory")
#define BAR() asm volatile("s_waitcnt lgkmcnt(0)\n\ts_barrier" ::: "memory")
#define WAITBAR(n) asm volatile("s_waitcnt vmcnt(%0) lgkmcnt(0)\n\ts_barrier" ::"n"(n) : "memory")

#define STAMP(i)
#define STAMP_DECL
#define STAMP_FLUSH()

#define NOSC1 0
template <int SC1>
DI void dma16(const void* base, u32 voff, u32 lds) {
    if constexpr (SC1 && !NOSC1) asm volatile("s_mov_b32 m0, %2\n\ts_nop 0\n\tglobal_load_lds_dwordx4 %0, %1 sc1" ::"v"(voff), "s"(base), "s"(lds) : "memory");
    else asm volatile("s_mov_b32 m0, %2\n\ts_nop 0\n\tglobal_load_lds_dwordx4 %0, %1" ::"v"(voff), "s"(base), "s"(lds) : "memory");
}
DI u32 ld32_sc1(const void* b, u32 o) { u32 v; asm volatile("global_load_dword %0, %1, %2 sc1" : "=v"(v) : "v"(o), "s"(b) : "memory"); return v; }
DI u32 ld16(const void* b, u32 o) { u32 v; asm volatile("global_load_ushort %0, %1, %2" : "=v"(v) : "v"(o), "s"(b) : "memory"); return v; }
DI u32 ld32(const void* b, u32 o) { u32 v; asm volatile("global_load_dword %0, %1, %2" : "=v"(v) : "v"(o), "s"(b) : "memory"); return v; }
DI u32x2 ld64_sc1(const void* b, u32 o) { u32x2 v; asm volatile("global_load_dwordx2 %0, %1, %2 sc1" : "=v"(v) : "v"(o), "s"(b) : "memory"); return v; }
DI u32x4 ld128_sc1(const void* b, u32 o) { u32x4 v; asm volatile("global_load_dwordx4 %0, %1, %2 sc1" : "=v"(v) : "v"(o), "s"(b) : "memory"); return v; }
DI void st32_sc1(void* b, u32 o, u32 v) { asm volatile("global_store_dword %0, %1, %2 sc1" ::"v"(o), "v"(v), "s"(b) : "memory"); }
DI void st64_sc1(void* b, u32 o, u32x2 v) { asm volatile("global_store_dwordx2 %0, %1, %2 sc1\n\ts_nop 1" ::"v"(o), "v"(v), "s"(b) : "memory"); }
DI void st128_sc1(void* b, u32 o, u32x4 v) { asm volatile("global_store_dwordx4 %0, %1, %2 sc1\n\ts_nop 1" ::"v"(o), "v"(v), "s"(b) : "memory"); }
template <int N>
DI u32 take32(u32 v) { u32 o; asm volatile("s_waitcnt vmcnt(%2)\n\tv_mov_b32 %0, %1" : "=v"(o) : "v"(v), "n"(N) : "memory"); return o; }

DI void poll32(const void* b, u32 o, u32 tag) {
#pragma unroll 1
    for (int it = 0; it < SPIN_CAP; ++it) {
        u32 v;
        asm volatile("global_load_dword %0, %1, %2 sc1\n\ts_waitcnt vmcnt(0)" : "=v"(v) : "v"(o), "s"(b) : "memory");
        if (wall(v == tag)) break;
        __builtin_amdgcn_s_sleep(4);
    }
}
DI void poll32f(const void* b, u32 o, u32 tag) {
#pragma unroll 1
    for (int it = 0; it < SPIN_CAP; ++it) {
        u32 v;
        asm volatile("global_load_dword %0, %1, %2 sc1\n\ts_waitcnt vmcnt(0)" : "=v"(v) : "v"(o), "s"(b) : "memory");
        if (wall(v == tag)) break;
    }
}
DI void poll128(const void* b, u32 o, u32 tag) {
#pragma unroll 1
    for (int it = 0; it < SPIN_CAP; ++it) {
        u32x4 v;
        asm volatile("global_load_dwordx4 %0, %1, %2 sc1\n\ts_waitcnt vmcnt(0)" : "=v"(v) : "v"(o), "s"(b) : "memory");
        if (wall(v.x == tag && v.y == tag && v.z == tag && v.w == tag)) break;
        __builtin_amdgcn_s_sleep(4);
    }
}
DI bf16x8 trfrag(const char* a0, const char* a1) {
    s16x4 t0 = __builtin_amdgcn_ds_read_tr16_b64_v4i16((LDSP s16x4*)a0);
    s16x4 t1 = __builtin_amdgcn_ds_read_tr16_b64_v4i16((LDSP s16x4*)a1);
    return __builtin_bit_cast(bf16x8, __builtin_shufflevector(t0, t1, 0, 1, 2, 3, 4, 5, 6, 7));
}
DI f32x4 mfma(bf16x8 a, bf16x8 b, f32x4 c) { return __builtin_amdgcn_mfma_f32_16x16x32_bf16(a, b, c, 0, 0, 0); }
DI f32x4 mfmah(f16x8 a, f16x8 b, f32x4 c) { return __builtin_amdgcn_mfma_f32_16x16x32_f16(a, b, c, 0, 0, 0); }
DI f32x16 mfma32(bf16x8 a, bf16x8 b, f32x16 c) { return __builtin_amdgcn_mfma_f32_32x32x16_bf16(a, b, c, 0, 0, 0); }

struct Cplx { float r, i; };
DI Cplx cmul(Cplx a, Cplx b) { return {__builtin_fmaf(a.r, b.r, -(a.i * b.i)), __builtin_fmaf(a.r, b.i, a.i * b.r)}; }
DI Cplx csq(Cplx a) { return {__builtin_fmaf(a.r, a.r, -(a.i * a.i)), (a.r + a.r) * a.i}; }

constexpr int g1_ops(int n) { return ((n >= 1 && n <= 16) ? 1 : 0) + ((n >= 2 && n <= 15) ? 1 : 0); }
constexpr int g1_allow(int n) {
    if (n == 0) return 7;
    if (n == 1) return 6;
    if (n == 2) return 3 + g1_ops(1);
    if (n == 3) return 2 + g1_ops(1) + g1_ops(2);
    if ((n & 1) == 0 && n >= 6) return g1_ops(n - 2) + g1_ops(n - 1);
    return (g1_ops(n - 3) - 1) + g1_ops(n - 2) + g1_ops(n - 1);
}
constexpr int g2_hi(int i) { int m = (10 * (i - 1) + 64) / 8; if (m < 8) m = 8; return m > 34 ? 34 : m; }
constexpr int g2_lo(int i) { return i == 0 ? 8 : g2_hi(i - 1); }
constexpr int g2_ops(int i, int AD) { return (g2_hi(i) - g2_lo(i)) + ((i + AD < 27) ? 1 : 0) + (i == 20 ? 2 : 0) + (i == 23 ? 1 : 0); }
constexpr int g2_allow(int I, int AD) {
    int a = 0;
    if (I < AD) { a = AD - 1 - I; for (int i = 0; i < I; ++i) a += g2_ops(i, AD); }
    else { a = g2_ops(I - AD, AD) - (g2_hi(I - AD) - g2_lo(I - AD)) - 1; for (int i = I - AD + 1; i < I; ++i) a += g2_ops(i, AD); }
    const int need = (10 * I + 9) / 8;
    if (need >= 8) {
        int it = 0;
        while (g2_hi(it) <= need) ++it;
        int b = g2_ops(it, AD) - (need - g2_lo(it)) - 1;
        for (int i = it + 1; i < I; ++i) b += g2_ops(i, AD);
        if (b < a) a = b;
    }
    return a;
}
#define SB() __builtin_amdgcn_sched_barrier(0)

__global__ __launch_bounds__(512, 4) void fused_kernel(
    const u16* __restrict__ x, const u16* __restrict__ Are, const u16* __restrict__ Aim, const u16* __restrict__ Bre,
    const u16* __restrict__ Bim, const u16* __restrict__ Cre, const u16* __restrict__ Cim, const u16* __restrict__ logdt,
    const u16* __restrict__ Dsk, const u16* __restrict__ w1, const u16* __restrict__ b1, const u16* __restrict__ w2,
    u16* __restrict__ out, char* __restrict__ ws, int mask) {
    extern __shared__ __attribute__((aligned(16))) char smem[];
    const int tid = __builtin_amdgcn_workitem_id_x();
    const int l = tid & 63;
    const int w = __builtin_amdgcn_readfirstlane(tid >> 6);
    const int g = l >> 4, c16 = l & 15;
    const int bid = __builtin_amdgcn_workgroup_id_x();
    const u32 lb = (u32)(size_t)(LDSP char*)smem;
    STAMP_DECL;
    STAMP(0);

    auto w2_load = [&](int ks, u32x4 (&wv)[5]) {
#pragma unroll
        for (int i = 0; i < 5; ++i) {
            const int n = i * 64 + l;
            wv[i] = *(const u32x4*)((const char*)w2 + (u32)((ks * 32 + n / 10) * 1280 + w * 160 + (n % 10) * 16));
        }
    };
    auto w2_store = [&](int ks, const u32x4 (&wv)[5]) {
        char* tile = smem + w * 5632;
#pragma unroll
        for (int i = 0; i < 5; ++i) {
            const int n = i * 64 + l;
            *(u32x4*)(tile + (n / 10) * 176 + (n % 10) * 16) = wv[i];
        }
#pragma unroll
        for (int dd = 0; dd < 5; ++dd) {
            const char* a = tile + (16 * (g >> 1) + 4 * (g & 1) + (c16 >> 2)) * 176 + dd * 32 + 8 * (l & 3);
            const bf16x8 f = trfrag(a, a + 8 * 176);
            st128_sc1(ws, WS_W2T + (u32)(((w * 54 + ks) * 5 + dd) * 1024 + l * 16), __builtin_bit_cast(u32x4, f));
        }
    };

    if (bid >= 256) {
        __builtin_amdgcn_s_setprio(2);
        const int gb = bid - 256;
        const int xcd = gb & 7, idx = gb >> 3;
        const int t2 = 8 * (xcd >> 1) + (idx & 7), cg = 4 * (xcd & 1) + (idx >> 3);
        u32 tag = 0;
        const bool has1 = (idx < 28) && (mask & 4);
        const bool has2 = (mask & 8) != 0;
        const int tt = 4 * (xcd >> 1) + (idx & 3);
        const int ct = 7 * (xcd & 1) + (idx >> 2);
        const int r32 = l & 31, h = l >> 5;
        const bool half = (ct == 13);
        float* csum = (float*)(smem + 73728);
        const u32 gw2 = WS_W2T + (u32)(cg * 54 * 5120 + l * 16);
        const int i0 = ((((idx & 7) * 27) >> 3) + (idx >> 3) * 7) % 27;
        auto rot = [&](int I) { return (I + i0 >= 27) ? I + i0 - 27 : I + i0; };

        const int pw = w & 3, th = pw & 1, fh = pw >> 1, tbe = w >> 2;
        const u32 stoff = WS_STAT + (u32)((tt * 128 + 64 * th + 32 * tbe + r32) * 8);
        auto pf_w2 = [&](int m) {
#pragma unroll
            for (int e = 0; e < 2; ++e) {
                int f = 10 * i0 + 8 * m + 2 * pw + e;
                if (f >= 270) f -= 270;
                dma16<1>(ws, gw2 + f * 1024, lb + m * 8192 + (2 * pw + e) * 1024);
            }
        };
        auto epilogue1 = [&](const f32x16& a0, const f32x16& a1, u32x2 st0, bool pf) {
#pragma unroll 1
            for (int it = 0; it < SPIN_CAP; ++it) {
                if (wall(st0.y == tag)) break;
                asm volatile("global_load_dwordx2 %0, %1, %2 sc1\n\ts_waitcnt vmcnt(0)" : "=v"(st0) : "v"(stoff), "s"(ws) : "memory");
            }
            STAMP(3);
            const float* cp = csum + fh * 64 + 4 * h;
            const u32 abase = WS_ACT + (u32)(((tt * 8 + 4 * th + 2 * tbe + (r32 >> 4)) * 54 + ct * 4 + fh * 2) * 1024 + (h * 16 + c16) * 16);
            const float nmt = -lo16(st0.x), rt = 1.44269504f * __builtin_amdgcn_rcpf(hi16(st0.x));
            auto epi = [&](const f32x16& a, int c, int g0) {
#pragma unroll
                for (int s = 0; s < 2; ++s) {
                    u32 pk[4];
#pragma unroll
                    for (int mm = 0; mm < 2; ++mm) {
                        const int m = 2 * s + mm;
                        const f32x4 c4 = *(const f32x4*)(cp + c * 32 + 8 * m);
                        const f32x4 b4 = *(const f32x4*)(cp + 128 + c * 32 + 8 * m);
                        float r[4];
#pragma unroll
                        for (int i = 0; i < 4; ++i) {
                            const float h2 = __builtin_fmaf(__builtin_fmaf(nmt, c4[i], a[4 * m + i]), rt, b4[i]);
                            const float e = __builtin_amdgcn_exp2f(h2);
                            const float rr = __builtin_amdgcn_rcpf(__builtin_fmaf(e, e + 2.f, 2.f));
                            r[i] = h2 * __builtin_fmaf(-1.38629436f, rr, 0.69314718f);
                        }
                        pk[2 * mm] = pk2(r[0], r[1]);
                        pk[2 * mm + 1] = pk2(r[2], r[3]);
                    }
                    st128_sc1(ws, abase + c * 1024 + s * 512, u32x4{pk[0], pk[1], pk[2], pk[3]});
                }
            };
            epi(a0, th, 0);
            epi(a1, th ^ 1, 4);
            STAMP(4);
            WAITV(0);
            STAMP(5);
            if (l == 0) st32_sc1(ws, WS_ACTF + (u32)(((tt * 4 + 2 * th + tbe) * 32 + ct * 2 + fh) * 4), tag);
        };
        char* hand = smem + 32768 + pw * 2048 + l * 16;
        bool pfd = false;
        const bool eact = !(half && fh == 1);

        if (w >= 4) {
            const int lw = w - 4;
            const u32 ev = ld32_sc1(ws, WS_EPOCH);
            if (has1) {
                u32 gx[2], gw[2];
#pragma unroll
                for (int e = 0; e < 2; ++e) {
                    const int vw = 2 * lw + e;
                    gx[e] = (u32)((tt * 128 + 8 * vw + (l >> 3)) * 1280 + (((l & 7) ^ ((4 * e + g) & 7)) << 4));
                    int wch = c16 ^ ((g << 2) | (vw & 3));
                    if (half) wch &= 7;
                    gw[e] = (u32)((4 * vw + g) * 3456 + ct * 256 + (wch << 4));
                }
                const u32 ldst = lb + lw * 2048;
                const int j0 = (3 * (idx >> 2) + ((5 * (idx & 3)) >> 1)) % 10;
                auto issueW = [&](auto NC) {
                    constexpr int N = decltype(NC)::value, j = N >> 1;
                    const int jr = (j + j0 >= 10) ? j + j0 - 10 : j + j0;
                    const u32 o = jr * (64 * 3456) + (N & 1) * (32 * 3456);
                    dma16<0>(w1, gw[0] + o, ldst + (N & 3) * 8192);
                    dma16<0>(w1, gw[1] + o, ldst + (N & 3) * 8192 + 1024);
                };
                auto issueX = [&](auto JC, auto UC) {
                    constexpr int j = decltype(JC)::value, U = decltype(UC)::value;
                    const int jr = (j + j0 >= 10) ? j + j0 - 10 : j + j0;
                    const u32 o = U * (64 * 1280) + jr * 128;
                    dma16<0>(x, gx[0] + o, ldst + 32768 + (j % 3) * 16384 + U * 8192);
                    dma16<0>(x, gx[1] + o, ldst + 32768 + (j % 3) * 16384 + U * 8192 + 1024);
                };
                issueW(IC<0>{}); issueX(IC<0>{}, IC<0>{}); issueX(IC<0>{}, IC<1>{}); issueW(IC<1>{}); issueW(IC<2>{});
                issueX(IC<1>{}, IC<0>{}); issueX(IC<1>{}, IC<1>{}); issueW(IC<3>{}); issueX(IC<2>{}, IC<0>{}); issueX(IC<2>{}, IC<1>{});
                u32 bv = 0, w2flag = 0;
                u32x2 st0;
                auto lstep = [&](auto NC) {
                    constexpr int N = decltype(NC)::value;
                    WAITBAR(2 * g1_allow(N) + (N >= 18 ? 3 : 0));
                    if constexpr (N >= 1 && N <= 16) issueW(IC<N + 3>{});
                    if constexpr (N >= 2 && N <= 15) issueX(IC<N / 2 + 2>{}, IC<N & 1>{});
                    if constexpr (N == 0) {
                        tag = mktag(take32<63>(ev));
                        if (tid == 256) st32_sc1(ws, WS_RD + bid * 4, tag);
                    }
                    if constexpr (N == 17) {
                        bv = ld16(b1, (u32)((ct * 128 + (tid & (half ? 63 : 127))) * 2));
                        st0 = ld64_sc1(ws, stoff);
                        w2flag = ld32_sc1(ws, WS_W2F + (u32)(cg * 256 + (l < 54 ? l : 0) * 4));
                    }
                };
                lstep(IC<0>{}); lstep(IC<1>{}); lstep(IC<2>{}); lstep(IC<3>{}); lstep(IC<4>{}); lstep(IC<5>{}); lstep(IC<6>{});
                lstep(IC<7>{}); lstep(IC<8>{}); lstep(IC<9>{}); lstep(IC<10>{}); lstep(IC<11>{}); lstep(IC<12>{}); lstep(IC<13>{});
                lstep(IC<14>{}); lstep(IC<15>{}); lstep(IC<16>{}); lstep(IC<17>{}); lstep(IC<18>{}); lstep(IC<19>{});
                asm volatile("s_waitcnt vmcnt(0)\n\ts_barrier" : "+v"(bv), "+v"(st0), "+v"(w2flag)::"memory");
                if (tid < 384) csum[128 + (tid & 127)] = bf2f(bv) * 1.44269504f;
                if (tid == 384) csum[256] = asf(tag);
                BAR();
                pfd = has2 && wall(w2flag == tag);
                if (eact) {
                    f32x16 a0, a1;
#pragma unroll
                    for (int i = 0; i < 4; ++i) {
                        const f32x4 u = *(const f32x4*)(hand + (i >> 1) * 8192 + (i & 1) * 1024);
                        const f32x4 v = *(const f32x4*)(hand + (2 + (i >> 1)) * 8192 + (i & 1) * 1024);
#pragma unroll
                        for (int e = 0; e < 4; ++e) { a0[4 * i + e] = u[e]; a1[4 * i + e] = v[e]; }
                    }
                    WAITL0();
                    epilogue1(a0, a1, st0, false);
                }
            } else {
                tag = mktag(take32<0>(ev));
            }
        } else if (has1) {
            const int xa = 32768 + th * 8192 + r32 * 128 + ((h ^ ((r32 >> 1) & 7)) << 4);
            int wa[2];
            {
                const int q = c16 >> 2, p = l & 3, blk = (l >> 4) & 1;
#pragma unroll
                for (int t = 0; t < 2; ++t) {
                    const int row = 8 * h + 4 * t + q;
                    const int f = (q << 2) | ((2 * h + t) & 3);
                    wa[t] = (row * 256 + (((2 * blk + (p >> 1)) ^ f) << 4) + 8 * (p & 1)) ^ (fh << 7) ^ (th << 6);
                }
            }
            f32x16 acc[2][2];
#pragma unroll
            for (int i = 0; i < 16; ++i) { acc[0][0][i] = 0.f; acc[0][1][i] = 0.f; acc[1][0][i] = 0.f; acc[1][1][i] = 0.f; }
            float cs = 0.f, cs2 = 0.f;
            u32x2 st0;
            const bf16x2 ones = __builtin_bit_cast(bf16x2, 0x3F803F80u);
            bf16x8 xf[2][2], wf[2][2];
            auto cstep = [&](auto MC) {
                constexpr int M = decltype(MC)::value, N = M >> 1, B = M & 1, P = B ^ 1;
                auto rdx = [&](int tb) { if constexpr (M < 40) xf[B][tb] = *(const bf16x8*)(smem + ((M >> 2) % 3) * 16384 + tb * 4096 + (xa ^ ((M & 3) << 5))); };
                auto rdw = [&](int c) {
                    if constexpr (M < 40) wf[B][c] = trfrag(smem + (N & 3) * 8192 + B * 4096 + (wa[0] ^ (c << 6)), smem + (N & 3) * 8192 + B * 4096 + (wa[1] ^ (c << 6)));
                };
                SB();
                if constexpr (B == 0) BAR();
                if constexpr (M == 0) STAMP(1);
                if constexpr (M >= 1) acc[0][0] = mfma32(wf[P][0], xf[P][0], acc[0][0]);
                SB();
                rdx(0); rdw(0);
                SB();
                if constexpr (M >= 1) acc[1][0] = mfma32(wf[P][0], xf[P][1], acc[1][0]);
                SB();
                rdx(1); rdw(1);
                SB();
                if constexpr (M >= 1) acc[0][1] = mfma32(wf[P][1], xf[P][0], acc[0][1]);
                SB();
                if constexpr (M >= 1) {
                    const u32x4 wv = __builtin_bit_cast(u32x4, wf[P][0]);
                    const u32 a0 = wv[0], a1 = wv[1], a2_ = wv[2], a3 = wv[3];
                    cs = __builtin_amdgcn_fdot2_f32_bf16(__builtin_bit_cast(bf16x2, a0), ones, cs, false);
                    cs2 = __builtin_amdgcn_fdot2_f32_bf16(__builtin_bit_cast(bf16x2, a1), ones, cs2, false);
                    cs = __builtin_amdgcn_fdot2_f32_bf16(__builtin_bit_cast(bf16x2, a2_), ones, cs, false);
                    cs2 = __builtin_amdgcn_fdot2_f32_bf16(__builtin_bit_cast(bf16x2, a3), ones, cs2, false);
                }
                if constexpr (M == 36) st0 = ld64_sc1(ws, stoff);
                SB();
                if constexpr (M >= 1) acc[1][1] = mfma32(wf[P][1], xf[P][1], acc[1][1]);
            };
#define C4(m) cstep(IC<m>{}); cstep(IC<m + 1>{}); cstep(IC<m + 2>{}); cstep(IC<m + 3>{});
            C4(0) C4(4) C4(8) C4(12) C4(16) C4(20) C4(24) C4(28) C4(32) C4(36)
#undef C4
            cstep(IC<40>{});
            STAMP(2);
            cs += cs2;
            cs += asf((u32)__builtin_amdgcn_ds_bpermute((l ^ 32) * 4, (int)asu(cs)));
            if (l < 32) csum[fh * 64 + th * 32 + l] = cs;
#pragma unroll
            for (int i = 0; i < 4; ++i) {
                *(f32x4*)(hand + (i >> 1) * 8192 + (i & 1) * 1024) = f32x4{acc[1][0][4 * i], acc[1][0][4 * i + 1], acc[1][0][4 * i + 2], acc[1][0][4 * i + 3]};
                *(f32x4*)(hand + (2 + (i >> 1)) * 8192 + (i & 1) * 1024) = f32x4{acc[1][1][4 * i], acc[1][1][4 * i + 1], acc[1][1][4 * i + 2], acc[1][1][4 * i + 3]};
            }
            asm volatile("s_waitcnt vmcnt(0) lgkmcnt(0)\n\ts_barrier" : "+v"(st0)::"memory");
            tag = asu(csum[256]);
            if (eact) epilogue1(acc[0][0], acc[0][1], st0, false);
        }
        if (idx >= 28) {
            if (w < 4) { const u32 ev = ld32_sc1(ws, WS_EPOCH); tag = mktag(take32<0>(ev)); }
            if (mask & 1) {
                const int k0 = gb - 224;
                u32x4 wv[5], wv2[5];
                w2_load(k0, wv);
                if (k0 < 22) w2_load(k0 + 32, wv2);
                w2_store(k0, wv);
                if (k0 < 22) w2_store(k0 + 32, wv2);
                WAITV(0);
                if (l == 0) st32_sc1(ws, WS_W2F + (u32)((w * 64 + k0) * 4), tag);
                if (k0 < 22 && l == 0) st32_sc1(ws, WS_W2F + (u32)((w * 64 + k0 + 32) * 4), tag);
            }
            BAR();
            if (tid == 0) st32_sc1(ws, WS_RD + bid * 4, tag);
        } else if (!has1 && w < 4) {
            const u32 ev = ld32_sc1(ws, WS_EPOCH);
            tag = mktag(take32<0>(ev));
        }

        if (has2) {
            const int tbk = w & 3, kp = w >> 2;
            BAR();
            if (w >= 4) {
                if (!pfd) poll32(ws, WS_W2F + (u32)(cg * 256 + (l < 54 ? l : 0) * 4), tag);
#pragma unroll
                for (int m = 0; m < 8; ++m) pf_w2(m);
            } else if (w == 0) poll32f(ws, WS_ACTF + (u32)((t2 * 64 + (r32 < 27 ? l : 0)) * 4), tag);
            int fsrc = 10 * i0 + w + 64;
            if (fsrc >= 270) fsrc -= 270;
            auto issue_w2 = [&](auto MC) {
                constexpr int M = decltype(MC)::value;
                if (M < 33 || w < 6) dma16<1>(ws, gw2 + fsrc * 1024, lb + (M & 7) * 8192 + w * 1024);
                else dma16<1>(ws, gw2, lb + 73728 + 2048 + (w & 1) * 1024);
                fsrc += 8;
                if (fsrc >= 270) fsrc -= 270;
            };
            WAITBAR(0);
            STAMP(6);
            const u32 ga = WS_ACT + (u32)(((4 * t2 + tbk) * 54 + kp) * 1024 + l * 16);
            constexpr int AD = 6;
            u32x4 af[8];
#pragma unroll
            for (int i = 0; i < AD; ++i) af[i] = ld128_sc1(ws, ga + rot(i) * 2048);
            f32x4 a2[5];
#pragma unroll
            for (int d = 0; d < 5; ++d) a2[d] = f32x4{0, 0, 0, 0};
            const int wl = kp * 5120 + l * 16;
            u32 sf0 = 0, sf1 = 0;
            bool sdone = false;
            const int n0 = w * 64 + l, n1 = 512 + (w & 1) * 64 + l;
            const u32 gx0 = (u32)((t2 * 64 + n0 / 10) * 1280 + cg * 160 + (n0 % 10) * 16);
            const u32 gx1 = (u32)((t2 * 64 + n1 / 10) * 1280 + cg * 160 + (n1 % 10) * 16);
            const u32 gs0 = WS_ST + (u32)((cg * 80 + (n0 >> 3)) * 4096 + t2 * 128 + (n0 & 7) * 16);
            const u32 gs1 = WS_ST + (u32)((cg * 80 + (n1 >> 3)) * 4096 + t2 * 128 + (n1 & 7) * 16);
            constexpr int XS = 16384, SS = 65536;
            const u32 sfp = WS_SF + (u32)(((t2 >> 4) * 640 + cg * 80) * 4);
            bf16x8 wq[2][5];
            auto load_s = [&]() {
                dma16<1>(ws, gs0, lb + SS + w * 1024);
                if (w < 2) dma16<1>(ws, gs1, lb + SS + 8192 + w * 1024);
            };
            auto st2 = [&](auto IC_) {
                constexpr int I = decltype(IC_)::value, B = I & 1;
                constexpr int allow = g2_allow(I, AD);
                SB();
                if constexpr (I == 24)
                    asm volatile("s_waitcnt vmcnt(%3)\n\ts_barrier" : "+v"(af[I & 7]), "+v"(sf0), "+v"(sf1) : "n"(allow < g2_ops(21, AD) + g2_ops(22, AD) + g2_ops(23, AD) ? allow : g2_ops(21, AD) + g2_ops(22, AD) + g2_ops(23, AD)) : "memory");
                else
                    asm volatile("s_waitcnt vmcnt(%1)\n\ts_barrier" : "+v"(af[I & 7]) : "n"(allow) : "memory");
                const bf16x8 bfrag = __builtin_bit_cast(bf16x8, af[(I + 7) & 7]);
#pragma unroll
                for (int d = 0; d < 5; ++d) {
                    if constexpr (I >= 1) a2[d] = mfma(wq[B ^ 1][d], bfrag, a2[d]);
                    SB();
                    {
                        const int o0 = ((10 * I + d) & 63) * 1024;
                        if (o0 + 5120 + 1024 <= 65536) wq[B][d] = *(const bf16x8*)(smem + wl + o0);
                        else wq[B][d] = *(const bf16x8*)(smem + ((wl + o0) & 65535));
                    }
                    if (d == 0) { if constexpr (g2_lo(I) < g2_hi(I)) issue_w2(IC<g2_lo(I)>{}); }
                    if (d == 1) { if constexpr (g2_lo(I) + 1 < g2_hi(I)) issue_w2(IC<g2_lo(I) + 1>{}); }
                    if (d == 2) { if constexpr (I + AD < 27) af[(I + AD) & 7] = ld128_sc1(ws, ga + rot(I + AD) * 2048); }
                    SB();
                }
                if constexpr (I == 20) {
                    sf0 = ld32_sc1(ws, sfp + l * 4);
                    sf1 = ld32_sc1(ws, sfp + 256 + c16 * 4);
                }
                if constexpr (I == 23) {
                    dma16<0>(x, gx0, lb + XS + w * 1024);
                    if (w < 2) dma16<0>(x, gx1, lb + XS + 8192 + w * 1024);
                }
                if constexpr (I == 24) {
                    sdone = wall(sf0 == tag && sf1 == tag);
                    if (sdone) load_s();
                }
            };
            st2(IC<0>{}); st2(IC<1>{}); st2(IC<2>{}); st2(IC<3>{}); st2(IC<4>{}); st2(IC<5>{}); st2(IC<6>{}); st2(IC<7>{}); st2(IC<8>{});
            st2(IC<9>{}); st2(IC<10>{}); st2(IC<11>{}); st2(IC<12>{}); st2(IC<13>{}); st2(IC<14>{}); st2(IC<15>{}); st2(IC<16>{});
            st2(IC<17>{}); st2(IC<18>{}); st2(IC<19>{}); st2(IC<20>{}); st2(IC<21>{}); st2(IC<22>{}); st2(IC<23>{}); st2(IC<24>{});
            st2(IC<25>{}); st2(IC<26>{});
            {
                const bf16x8 bfrag = __builtin_bit_cast(bf16x8, af[26 & 7]);
#pragma unroll
                for (int d = 0; d < 5; ++d) a2[d] = mfma(wq[0][d], bfrag, a2[d]);
            }
            STAMP(7);
            if (!sdone) {
                poll32(ws, sfp + l * 4, tag);
                poll32(ws, sfp + 256 + c16 * 4, tag);
                load_s();
            }
            char* ex = smem + 30720 + tbk * 5120 + l * 16;
            if (kp == 0) { *(f32x4*)(ex + 3072) = a2[3]; *(f32x4*)(ex + 4096) = a2[4]; }
            else { *(f32x4*)(ex) = a2[0]; *(f32x4*)(ex + 1024) = a2[1]; *(f32x4*)(ex + 2048) = a2[2]; }
            WAITBAR(0);
            STAMP(8);
            auto fin = [&](int d, f32x4 o) {
                o += *(const f32x4*)(ex + d * 1024);
                char* xp = smem + XS + (16 * tbk + c16) * 160 + d * 32 + 8 * g;
                const u32x2 xv = *(const u32x2*)xp;
                const s16x4 sv4 = __builtin_amdgcn_ds_read_tr16_b64_v4i16(
                    (LDSP s16x4*)(smem + SS + (16 * d + 4 * g + (c16 >> 2)) * 128 + tbk * 32 + 8 * (l & 3)));
                const u32x2 sv = __builtin_bit_cast(u32x2, sv4);
                const float xs[4] = {lo16(xv.x), hi16(xv.x), lo16(xv.y), hi16(xv.y)};
                const float ss[4] = {lo16(sv.x), hi16(sv.x), lo16(sv.y), hi16(sv.y)};
                float r[4];
#pragma unroll
                for (int i = 0; i < 4; ++i) {
                    const float sg = __builtin_amdgcn_rcpf(1.f + __builtin_amdgcn_exp2f(-1.44269504f * o[i]));
                    r[i] = __builtin_fmaf(ss[i] * o[i], sg, xs[i]);
                }
                *(u32x2*)xp = u32x2{pk2(r[0], r[1]), pk2(r[2], r[3])};
            };
            if (kp == 0) { fin(0, a2[0]); fin(1, a2[1]); fin(2, a2[2]); }
            else { fin(3, a2[3]); fin(4, a2[4]); }
            BAR();
            *(u32x4*)((char*)out + gx0) = *(const u32x4*)(smem + XS + n0 * 16);
            if (w < 2) *(u32x4*)((char*)out + gx1) = *(const u32x4*)(smem + XS + n1 * 16);
        }
        STAMP(9);
        if (mask & 12) STAMP_FLUSH();
        return;
    }

    const int cb = bid;
    const int cx = cb & 7, ci = cb >> 3;
    const int tg = 64 * (cx >> 1) + 32 * (cx & 1) + ci;
    const int tk = w >> 2;
    const int ch = (w >> 1) & 1, p = w & 1;
    const bool doconv = (mask & 2) && (tk == 0 || ci >= 24);
    const int d0 = 80 * cx + 2 * (tk == 0 ? ci : ci + 8);
    const int d = d0 + ch;
    u32 xv[5];
    if (mask & 1) {
        const u32* xr = (const u32*)x + (u32)((tg * 8 + w) * 320 + l);
#pragma unroll
        for (int i = 0; i < 5; ++i) xv[i] = xr[64 * i];
    }
    const u32 ev = ld32_sc1(ws, WS_EPOCH);
    u32 pa[6] = {0, 0, 0, 0, 0, 0}, pld = 0, pdk = 0;
    if (doconv) {
        const u32 pi = (u32)(d * NM + p * 64 + l);
        pa[0] = Are[pi]; pa[1] = Aim[pi]; pa[2] = Bre[pi]; pa[3] = Bim[pi]; pa[4] = Cre[pi]; pa[5] = Cim[pi];
        pld = logdt[d]; pdk = Dsk[d];
    }
    char* lnt = smem + 65536;
    float mb = 0.f, den = 1.f;
    if (mask & 1) {
        float s = 0.f;
#pragma unroll
        for (int i = 0; i < 5; ++i) s += lo16(xv[i]) + hi16(xv[i]);
        const float mean = wave_sum(s) * (1.f / 640.f);
        float q = 0.f;
#pragma unroll
        for (int i = 0; i < 5; ++i) {
            const float a = lo16(xv[i]) - mean, b = hi16(xv[i]) - mean;
            q = __builtin_fmaf(a, a, q); q = __builtin_fmaf(b, b, q);
        }
        const float var = wave_sum(q) * (1.f / 640.f);
        mb = rbf(mean);
        den = rbf(__builtin_sqrtf(rbf(rbf(var) + 1e-5f)));
        const float rden = 1.0f / den;
        char* lw = lnt + w * 1280 + l * 4 + w * 64;
#pragma unroll
        for (int i = 0; i < 5; ++i) {
            const u32 dd = pk2(lo16(xv[i]) - mb, hi16(xv[i]) - mb);
            const u32 o = pk2(lo16(dd) * rden, hi16(dd) * rden);
            if (i < 3) *(u32*)(lw + 256 * i) = o;
            else *(u32*)(lw + 256 * i - ((256 * i + l * 4 + w * 64 >= 1280) ? 1280 : 0)) = o;
        }
    }
    const u32 tag = mktag(doconv ? take32<8>(ev) : take32<0>(ev));
    STAMP(1);
    if ((mask & 1) && l == 0) st64_sc1(ws, WS_STAT + (u32)((tg * 8 + w) * 8), u32x2{pk2(mb, den), tag});
    BAR();
    if (tid == 0) st32_sc1(ws, WS_RD + bid * 4, tag);
    if (mask & 1) {
        const int q = c16 >> 2;
        auto piece = [&](int i) {
            int p0 = 2 * (i * 4 + g) + 4 * q, p1 = p0 + 16;
            if (p0 >= 80) p0 -= 80;
            if (p1 >= 80) p1 -= 80;
            const bf16x8 f = trfrag(lnt + q * 1280 + p0 * 16 + 8 * (l & 3), lnt + (q + 4) * 1280 + p1 * 16 + 8 * (l & 3));
            st128_sc1(ws, WS_XB + (u32)(tg * 10240 + i * 1024 + l * 16), __builtin_bit_cast(u32x4, f));
        };
        piece(w);
        if (w >= 6) piece(2 + w);
    }
    STAMP(2);
    if (!doconv) {
        WAITBAR(0);
        if (!(mask & 2) && (mask & 1) && tid == 0) st32_sc1(ws, WS_XBF + cb * 4, tag);
        if (cb == 0 && w == 7) {
            poll128(ws, WS_RD + l * 16, tag);
            poll128(ws, WS_RD + 1024 + l * 16, tag);
            if (l == 0) st32_sc1(ws, WS_EPOCH, tag);
        }
        return;
    }
    {
        const float dt = rbf(__builtin_expf(bf2f(pld)));
        const float lr2 = (dt * bf2f(pa[0])) * 1.44269504f;
        const float li = dt * bf2f(pa[1]);
        const float rh = li * 0.15915494f;
        const float rl = __builtin_fmaf(li, 0.15915494f, -rh) + li * 3.0908620e-9f;
        auto E = [&](float t) {
            const float ph = rh * t;
            const float er = __builtin_fmaf(rl, t, __builtin_fmaf(rh, t, -ph));
            const float fr = (ph - __builtin_rintf(ph)) + er;
            const float m = __builtin_amdgcn_exp2f(lr2 * t);
            return Cplx{m * __builtin_amdgcn_cosf(fr), m * __builtin_amdgcn_sinf(fr)};
        };
        char* scr = smem + w * 4608;
        auto put = [&](int row, Cplx v) { *(u32*)(scr + row * 288 + l * 4) = pkh(v.r, v.i); };
        const char* frd = scr + c16 * 288 + g * 16;
        f16x8 bq[4];
        float inv_scale;
        {
            Cplx c = cmul(Cplx{bf2f(pa[4]), bf2f(pa[5])}, Cplx{dt * bf2f(pa[2]), dt * bf2f(pa[3])});
            const float mx = wave_max(__builtin_fmaxf(__builtin_fabsf(c.r), __builtin_fabsf(c.i)));
            int ex = 14 - __builtin_amdgcn_frexp_expf(mx);
            ex = ex > 100 ? 100 : (ex < -100 ? -100 : ex);
            inv_scale = __builtin_amdgcn_ldexpf(1.f, -ex);
            c.r = __builtin_amdgcn_ldexpf(c.r, ex);
            c.i = -__builtin_amdgcn_ldexpf(c.i, ex);
            Cplx e8 = E(8.f);
            e8.i = -e8.i;
            const Cplx e16 = csq(e8), e32 = csq(e16), e64 = csq(e32);
            Cplx b[8];
            b[0] = c; b[1] = cmul(c, e8); b[2] = cmul(c, e16); b[3] = cmul(b[1], e16);
#pragma unroll
            for (int j = 0; j < 4; ++j) b[4 + j] = cmul(b[j], e32);
#pragma unroll
            for (int j = 0; j < 8; ++j) { put(j, b[j]); put(8 + j, cmul(b[j], e64)); }
#pragma unroll
            for (int s = 0; s < 4; ++s) bq[s] = *(const f16x8*)(frd + s * 64);
        }
        WAITBAR(0);
        if (tid == 0 && (mask & 1)) st32_sc1(ws, WS_XBF + cb * 4, tag);
        u32 ix[9];
#pragma unroll
        for (int k = 0; k < 9; ++k) ix[k] = g_idx.v[k * 128 + p * 64 + l];
        auto tile_mm = [&]() {
            f32x4 c = {0, 0, 0, 0};
#pragma unroll
            for (int s = 0; s < 4; ++s) c = mfmah(*(const f16x8*)(frd + s * 64), bq[s], c);
            return c;
        };
        f32x4 T0, T1, T2;
        {
            const Cplx e1 = E(1.f), e2 = csq(e1), e3 = cmul(e2, e1), e4 = csq(e2), e5 = cmul(e4, e1), e6 = cmul(e4, e2), e7 = cmul(e4, e3);
            const Cplx e128 = E(128.f);
            put(0, Cplx{1.f, 0.f}); put(1, e1); put(2, e2); put(3, e3); put(4, e4); put(5, e5); put(6, e6); put(7, e7);
            put(8, e128); put(9, cmul(e128, e1)); put(10, cmul(e128, e2)); put(11, cmul(e128, e3));
            put(12, cmul(e128, e4)); put(13, cmul(e128, e5)); put(14, cmul(e128, e6)); put(15, cmul(e128, e7));
            T0 = tile_mm();
            const Cplx e256 = csq(e128), e384 = cmul(e256, e128), e512 = csq(e256);
            const Cplx e640 = cmul(e512, e128), e768 = cmul(e512, e256), e896 = cmul(e512, e384);
            put(0, e256); put(1, cmul(e256, e2)); put(2, cmul(e256, e4)); put(3, cmul(e256, e6));
            put(4, e384); put(5, cmul(e384, e2)); put(6, cmul(e384, e4)); put(7, cmul(e384, e6));
            put(8, e512); put(9, cmul(e512, e4)); put(10, e640); put(11, cmul(e640, e4));
            put(12, e768); put(13, cmul(e768, e4)); put(14, e896); put(15, cmul(e896, e4));
            T1 = tile_mm();
            const Cplx e1k = E(1024.f);
            put(0, e1k); put(1, cmul(e1k, e128)); put(2, cmul(e1k, e256)); put(3, cmul(e1k, e384)); put(4, cmul(e1k, e512));
            put(5, cmul(e1k, e640)); put(6, cmul(e1k, e768)); put(7, cmul(e1k, e896)); put(8, csq(e1k));
            T2 = tile_mm();
        }
        STAMP(3);
        {
            float* tabp = (float*)scr;
            const int j = c16;
            *(f32x4*)(tabp + 128 * (g >> 1) + 4 * (g & 1) + 8 * j) = T0 * inv_scale;
            T1 *= inv_scale;
            T2 *= inv_scale;
            if (g < 2) {
                *(f32x4*)(tabp + 256 + 64 * g + 4 * j) = T1;
#pragma unroll
                for (int i = 0; i < 4; ++i) tabp[512 + 16 * (4 * g + i) + j] = T2[i];
            } else {
                *(f32x2*)(tabp + 384 + 64 * (g - 2) + 2 * j) = f32x2{T1[0], T1[1]};
                *(f32x2*)(tabp + 384 + 64 * (g - 2) + 32 + 2 * j) = f32x2{T1[2], T1[3]};
                if (g == 2) tabp[640 + j] = T2[0];
            }
        }
        if (w == 0) poll128(ws, WS_XBF + l * 16, tag);
        BAR();
        STAMP(4);
        char* U = smem + 36864 + tk * 9216;
#pragma unroll
        for (int i = 0; i < 2; ++i) {
            const int c = ((w & 3) * 2 + i) * 32 + (l >> 1);
            const int chs = (l & 1) ^ ((c >> 3) & 1);
            dma16<1>(ws, WS_XB + (u32)(c * 10240 + (d0 + chs) * 16), lb + 36864 + tk * 9216 + 1024 + ((w & 3) * 2 + i) * 1024);
        }
        if ((w & 3) == 0) *(u32x4*)(U + l * 16) = u32x4{0, 0, 0, 0};
        char* tab = smem + 76288 + (w >> 1) * 1344;
        {
            const float* t0 = (const float*)(smem + (w & 6) * 4608);
            const float* t1 = (const float*)(smem + (w | 1) * 4608);
#pragma unroll
            for (int it = 0; it < 3; ++it) {
                const int i = it * 128 + p * 64 + l;
                if (i < 328) {
                    const f32x2 a = *(const f32x2*)(t0 + 2 * i), b = *(const f32x2*)(t1 + 2 * i);
                    *(u32*)(tab + 4 * i) = pk2(a.x + b.x, a.y + b.y);
                } else if (i == 330) *(u32*)(tab + 4 * i) = 0u;
            }
        }
        BAR();
        STAMP(5);
        char* KE = smem + (w >> 1) * 8448;
        char* KO = KE + 4160;
        {
            const int m = p * 64 + l;
            u32 e[9];
#pragma unroll
            for (int k = 0; k < 9; ++k) {
                u16x2 v;
                v.x = *(const u16*)(tab + (ix[k] & 0xFFFFu));
                v.y = *(const u16*)(tab + (ix[k] >> 16));
                e[k] = __builtin_bit_cast(u32, v);
            }
            *(u32x4*)(KE + m * 32) = u32x4{e[0], e[1], e[2], e[3]};
            *(u32x4*)(KE + m * 32 + 16) = u32x4{e[4], e[5], e[6], e[7]};
            u32 o[8];
#pragma unroll
            for (int k = 0; k < 8; ++k) o[k] = __builtin_amdgcn_alignbit(e[k + 1], e[k], 16);
            *(u32x4*)(KO + m * 32) = u32x4{o[0], o[1], o[2], o[3]};
            *(u32x4*)(KO + m * 32 + 16) = u32x4{o[4], o[5], o[6], o[7]};
            if (p == 0 && l < 4) *(u32x4*)(KE + 4096 + (l & 1) * 16 + (l >> 1) * 4160) = u32x4{0, 0, 0, 0};
        }
        WAITBAR(0);
        STAMP(6);
        f32x4 y[8];
#pragma unroll
        for (int a = 0; a < 8; ++a) y[a] = f32x4{0, 0, 0, 0};
#pragma unroll 1
        for (int rr = 0; rr < 4; ++rr) {
            const int r = 8 * p + 2 * rr;
            const int c = 2 * (c16 - r - 1) + g + 32;
            const char* ub = U + c * 32 + ((ch ^ ((c >> 3) & 1)) << 4);
            bf16x8 B[8];
#pragma unroll
            for (int a = 0; a < 8; ++a) B[a] = *(const bf16x8*)(ub + a * 1024);
            const int i0 = 2031 - 16 * r - c16 + 8 * g;
            const char* kb = ((i0 & 1) ? KO : KE) + (i0 >> 1) * 4;
#pragma unroll
            for (int q = 0; q < 8; ++q) {
                const u32x4a av = *(const u32x4a*)(kb - q * 512);
                const bf16x8 A = __builtin_bit_cast(bf16x8, av);
#pragma unroll
                for (int a = q; a < 8; ++a) y[a] = mfma(A, B[a - q], y[a]);
            }
        }
        STAMP(7);
        const float dsk = bf2f(pdk);
        BAR();
        char* ew = smem + w * 4096 + l * 16;
        const char* eo = smem + (w ^ 1) * 4096 + l * 16;
        char* so = smem + 55296 + w * 2048;
        auto fin = [&](int a, f32x4 yy) {
            yy += *(const f32x4*)(eo + a * 1024);
            const int cu = 2 * (16 * (4 * p + a) + c16) + (g >> 1) + 32;
            const u32x2 uv = *(const u32x2*)(U + cu * 32 + ((ch ^ ((cu >> 3) & 1)) << 4) + (g & 1) * 8);
            *(u32x2*)(so + a * 512 + c16 * 32 + g * 8) =
                u32x2{pk2(__builtin_fmaf(lo16(uv.x), dsk, yy[0]), __builtin_fmaf(hi16(uv.x), dsk, yy[1])),
                      pk2(__builtin_fmaf(lo16(uv.y), dsk, yy[2]), __builtin_fmaf(hi16(uv.y), dsk, yy[3]))};
        };
        if (p == 0) {
#pragma unroll
            for (int a = 0; a < 4; ++a) *(f32x4*)(ew + a * 1024) = y[4 + a];
            BAR();
#pragma unroll
            for (int a = 0; a < 4; ++a) fin(a, y[a]);
        } else {
#pragma unroll
            for (int a = 0; a < 4; ++a) *(f32x4*)(ew + a * 1024) = y[a];
            BAR();
#pragma unroll
            for (int a = 0; a < 4; ++a) fin(a, y[4 + a]);
        }
#pragma unroll
        for (int i = 0; i < 2; ++i) {
            const u32x4 v = *(const u32x4*)(so + i * 1024 + l * 16);
            st128_sc1(ws, WS_ST + (u32)(d * 4096 + p * 2048 + i * 1024 + l * 16), v);
        }
        STAMP(8);
        WAITV(0);
        if (l == 0) st32_sc1(ws, WS_SF + (u32)((p * 640 + d) * 4), tag);
        STAMP(9);
        STAMP_FLUSH();
    }
}
#if defined(__HIP_DEVICE_COMPILE__)
#pragma clang attribute pop
#endif

extern "C" void kernel_launch(void* const* d_in, const int* in_sizes, int n_in, void* d_out, int out_size, void* d_ws,
                              size_t ws_size, hipStream_t stream) {
    (void)hipFuncSetAttribute((const void*)fused_kernel, hipFuncAttributeMaxDynamicSharedMemorySize, LDS_BYTES);
#define A(i) (const u16*)d_in[i]
    fused_kernel<<<512, 512, LDS_BYTES, stream>>>(A(0), A(1), A(2), A(3), A(4), A(5), A(6), A(7), A(8), A(10), A(11), A(12),
                                                  (u16*)d_out, (char*)d_ws, 15);
}
```

```cpp
#include <hip/hip_runtime.h>
#include <stdint.h>
#include <type_traits>
#include <initializer_list>

typedef unsigned short u16;
typedef unsigned int u32;
typedef unsigned long long u64;
typedef __attribute__((ext_vector_type(8))) __bf16 bf16x8;
typedef __attribute__((ext_vector_type(2))) __bf16 bf16x2;
typedef __attribute__((ext_vector_type(8))) _Float16 f16x8;
typedef __attribute__((ext_vector_type(2))) _Float16 f16x2;
typedef __attribute__((ext_vector_type(4))) short s16x4;
typedef __attribute__((ext_vector_type(2))) u16 u16x2;
typedef __attribute__((ext_vector_type(4))) float f32x4;
typedef __attribute__((ext_vector_type(16))) float f32x16;
typedef __attribute__((ext_vector_type(2))) float f32x2;
typedef __attribute__((ext_vector_type(4))) u32 u32x4;
typedef __attribute__((ext_vector_type(2))) u32 u32x2;
typedef __attribute__((ext_vector_type(4), aligned(4))) u32 u32x4a;
#define LDSP __attribute__((address_space(3)))
#define DI __device__ __forceinline__
template <int N> using IC = std::integral_constant<int, N>;

#define MULTI 0

#define WARM 0
#define TLBP 0
#define DELAY 0
#define NT1 0
#define NT2 0
#define W2DELAY 0
#define PSL1 8
#define PSL2 16
#define CPRIO 0
#define SWAPR 0
#define ROT1 0
#define ROT2 0
constexpr int NM = 128;
constexpr u32 WS_EPOCH = 0, WS_RD = 1024, WS_XBF = 4096, WS_W2F = 5120, WS_SF = 8192, WS_ACTF = 16384, WS_STAT = 32768;
constexpr u32 WS_XB = 1u << 20, WS_ST = 4u << 20, WS_W2T = 8u << 20, WS_ACT = 12u << 20;
constexpr int LDS_BYTES = 81920;
constexpr int SPIN_CAP = 1 << 16;

struct IdxTab {
    u32 v[9 * 128];
    static constexpr int idx(int l) {
        if (l < 0) return 660;
        if (l < 256) return l;
        int sh = l < 512 ? 1 : (l < 1024 ? 2 : 3);
        int q = (l + ((1 << (sh - 1)) - 1) + ((l >> sh) & 1)) >> sh;
        return 128 * sh + q;
    }
    constexpr IdxTab() : v() {
        for (int k = 0; k < 9; ++k)
            for (int m = 0; m < 128; ++m)
                v[k * 128 + m] = (u32)(2 * idx(2047 - (16 * m + 2 * k))) | ((u32)(2 * idx(2047 - (16 * m + 2 * k + 1))) << 16);
    }
};
__device__ const IdxTab g_idx = IdxTab();

#if defined(__HIP_DEVICE_COMPILE__)
#pragma clang attribute push(__attribute__((target("no-packed-fp32-ops"))), apply_to = function)
#endif
DI float asf(u32 v) { return __builtin_bit_cast(float, v); }
DI u32 asu(float v) { return __builtin_bit_cast(u32, v); }
DI bool wall(bool p) { return __builtin_amdgcn_ballot_w64(p) == ~0ull; }
DI float bf2f(u32 v) { return asf(v << 16); }
DI u32 pk2(float a, float b) { f32x2 f = {a, b}; return __builtin_bit_cast(u32, __builtin_convertvector(f, bf16x2)); }
DI u32 pkh(float a, float b) { f32x2 f = {a, b}; return __builtin_bit_cast(u32, __builtin_convertvector(f, f16x2)); }
DI float rbf(float a) { return asf(pk2(a, a) << 16); }
DI float lo16(u32 v) { return asf(v << 16); }
DI float hi16(u32 v) { return asf(v & 0xFFFF0000u); }
DI u32 mktag(u32 e) { return e * 0x9E3779B1u + 0x7F4A7C15u; }
template <int CTRL, int RM>
DI float dpp(float v) { return asf((u32)__builtin_amdgcn_update_dpp(0, (int)asu(v), CTRL, RM, 0xf, true)); }
DI float wave_sum(float v) {
    v += dpp<0x111, 0xf>(v); v += dpp<0x112, 0xf>(v); v += dpp<0x114, 0xf>(v); v += dpp<0x118, 0xf>(v);
    v += dpp<0x142, 0xa>(v); v += dpp<0x143, 0xc>(v);
    return asf((u32)__builtin_amdgcn_readlane((int)asu(v), 63));
}
DI float wave_max(float v) {
    v = __builtin_fmaxf(v, dpp<0x111, 0xf>(v)); v = __builtin_fmaxf(v, dpp<0x112, 0xf>(v));
    v = __builtin_fmaxf(v, dpp<0x114, 0xf>(v)); v = __builtin_fmaxf(v, dpp<0x118, 0xf>(v));
    v = __builtin_fmaxf(v, dpp<0x142, 0xa>(v)); v = __builtin_fmaxf(v, dpp<0x143, 0xc>(v));
    return asf((u32)__builtin_amdgcn_readlane((int)asu(v), 63));
}

#define WAITV(n) asm volatile("s_waitcnt vmcnt(%0)" ::"n"(n) : "memory")
#define WAITL0() asm volatile("s_waitcnt lgkmcnt(0)" ::: "memory")
#define BAR() asm volatile("s_waitcnt lgkmcnt(0)\n\ts_barrier" ::: "memory")
#define WAITBAR(n) asm volatile("s_waitcnt vmcnt(%0) lgkmcnt(0)\n\ts_barrier" ::"n"(n) : "memory")

#define STAMP(i)
#define STAMP_DECL
#define STAMP_FLUSH()

#define NOSC1 0
template <int SC1>
DI void dma16(const void* base, u32 voff, u32 lds) {
    if constexpr (SC1 && !NOSC1) asm volatile("s_mov_b32 m0, %2\n\ts_nop 0\n\tglobal_load_lds_dwordx4 %0, %1 sc1" ::"v"(voff), "s"(base), "s"(lds) : "memory");
    else if constexpr (NT1) asm volatile("s_mov_b32 m0, %2\n\ts_nop 0\n\tglobal_load_lds_dwordx4 %0, %1 nt" ::"v"(voff), "s"(base), "s"(lds) : "memory");
    else asm volatile("s_mov_b32 m0, %2\n\ts_nop 0\n\tglobal_load_lds_dwordx4 %0, %1" ::"v"(voff), "s"(base), "s"(lds) : "memory");
}
DI u32 ld32_sc1(const void* b, u32 o) { u32 v; asm volatile("global_load_dword %0, %1, %2 sc1" : "=v"(v) : "v"(o), "s"(b) : "memory"); return v; }
DI u32 ld16(const void* b, u32 o) { u32 v; asm volatile("global_load_ushort %0, %1, %2" : "=v"(v) : "v"(o), "s"(b) : "memory"); return v; }
DI u32 ld32(const void* b, u32 o) { u32 v; asm volatile("global_load_dword %0, %1, %2" : "=v"(v) : "v"(o), "s"(b) : "memory"); return v; }
DI u32x2 ld64_sc1(const void* b, u32 o) { u32x2 v; asm volatile("global_load_dwordx2 %0, %1, %2 sc1" : "=v"(v) : "v"(o), "s"(b) : "memory"); return v; }
DI u32x4 ld128_sc1(const void* b, u32 o) { u32x4 v; asm volatile("global_load_dwordx4 %0, %1, %2 sc1" : "=v"(v) : "v"(o), "s"(b) : "memory"); return v; }
DI void st32_sc1(void* b, u32 o, u32 v) { asm volatile("global_store_dword %0, %1, %2 sc1" ::"v"(o), "v"(v), "s"(b) : "memory"); }
DI void st64_sc1(void* b, u32 o, u32x2 v) { asm volatile("global_store_dwordx2 %0, %1, %2 sc1\n\ts_nop 1" ::"v"(o), "v"(v), "s"(b) : "memory"); }
DI void st128_sc1(void* b, u32 o, u32x4 v) { asm volatile("global_store_dwordx4 %0, %1, %2 sc1\n\ts_nop 1" ::"v"(o), "v"(v), "s"(b) : "memory"); }
template <int N>
DI u32 take32(u32 v) { u32 o; asm volatile("s_waitcnt vmcnt(%2)\n\tv_mov_b32 %0, %1" : "=v"(o) : "v"(v), "n"(N) : "memory"); return o; }

DI void poll32(const void* b, u32 o, u32 tag) {
#pragma unroll 1
    for (int it = 0; it < SPIN_CAP; ++it) {
        u32 v;
        asm volatile("global_load_dword %0, %1, %2 sc1\n\ts_waitcnt vmcnt(0)" : "=v"(v) : "v"(o), "s"(b) : "memory");
        if (wall(v == tag)) break;
        __builtin_amdgcn_s_sleep(PSL2);
    }
}
DI void poll32f(const void* b, u32 o, u32 tag) {
#pragma unroll 1
    for (int it = 0; it < SPIN_CAP; ++it) {
        u32 v;
        asm volatile("global_load_dword %0, %1, %2 sc1\n\ts_waitcnt vmcnt(0)" : "=v"(v) : "v"(o), "s"(b) : "memory");
        if (wall(v == tag)) break;
        if (PSL1) __builtin_amdgcn_s_sleep(PSL1);
    }
}
DI void poll128(const void* b, u32 o, u32 tag) {
#pragma unroll 1
    for (int it = 0; it < SPIN_CAP; ++it) {
        u32x4 v;
        asm volatile("global_load_dwordx4 %0, %1, %2 sc1\n\ts_waitcnt vmcnt(0)" : "=v"(v) : "v"(o), "s"(b) : "memory");
        if (wall(v.x == tag && v.y == tag && v.z == tag && v.w == tag)) break;
        __builtin_amdgcn_s_sleep(PSL2);
    }
}
DI bf16x8 trfrag(const char* a0, const char* a1) {
    s16x4 t0 = __builtin_amdgcn_ds_read_tr16_b64_v4i16((LDSP s16x4*)a0);
    s16x4 t1 = __builtin_amdgcn_ds_read_tr16_b64_v4i16((LDSP s16x4*)a1);
    return __builtin_bit_cast(bf16x8, __builtin_shufflevector(t0, t1, 0, 1, 2, 3, 4, 5, 6, 7));
}
DI f32x4 mfma(bf16x8 a, bf16x8 b, f32x4 c) { return __builtin_amdgcn_mfma_f32_16x16x32_bf16(a, b, c, 0, 0, 0); }
DI f32x4 mfmah(f16x8 a, f16x8 b, f32x4 c) { return __builtin_amdgcn_mfma_f32_16x16x32_f16(a, b, c, 0, 0, 0); }
DI f32x16 mfma32(bf16x8 a, bf16x8 b, f32x16 c) { return __builtin_amdgcn_mfma_f32_32x32x16_bf16(a, b, c, 0, 0, 0); }

struct Cplx { float r, i; };
DI Cplx cmul(Cplx a, Cplx b) { return {__builtin_fmaf(a.r, b.r, -(a.i * b.i)), __builtin_fmaf(a.r, b.i, a.i * b.r)}; }
DI Cplx csq(Cplx a) { return {__builtin_fmaf(a.r, a.r, -(a.i * a.i)), (a.r + a.r) * a.i}; }

constexpr int g1_ops(int n) { return ((n >= 1 && n <= 16) ? 1 : 0) + ((n >= 2 && n <= 15) ? 1 : 0); }
constexpr int g1_allow(int n) {
    if (n == 0) return 7;
    if (n == 1) return 6;
    if (n == 2) return 3 + g1_ops(1);
    if (n == 3) return 2 + g1_ops(1) + g1_ops(2);
    if ((n & 1) == 0 && n >= 6) return g1_ops(n - 2) + g1_ops(n - 1);
    return (g1_ops(n - 3) - 1) + g1_ops(n - 2) + g1_ops(n - 1);
}
constexpr int g2_hi(int i) { int m = (10 * (i - 1) + 64) / 8; if (m < 8) m = 8; return m > 34 ? 34 : m; }
constexpr int g2_lo(int i) { return i == 0 ? 8 : g2_hi(i - 1); }
constexpr int g2_ops(int i, int AD) { return (g2_hi(i) - g2_lo(i)) + ((i + AD < 27) ? 1 : 0) + (i == 20 ? 2 : 0) + (i == 23 ? 1 : 0); }
constexpr int g2_allow(int I, int AD) {
    int a = 0;
    if (I < AD) { a = AD - 1 - I; for (int i = 0; i < I; ++i) a += g2_ops(i, AD); }
    else { a = g2_ops(I - AD, AD) - (g2_hi(I - AD) - g2_lo(I - AD)) - 1; for (int i = I - AD + 1; i < I; ++i) a += g2_ops(i, AD); }
    const int need = (10 * I + 9) / 8;
    if (need >= 8) {
        int it = 0;
        while (g2_hi(it) <= need) ++it;
        int b = g2_ops(it, AD) - (need - g2_lo(it)) - 1;
        for (int i = it + 1; i < I; ++i) b += g2_ops(i, AD);
        if (b < a) a = b;
    }
    return a;
}
#define SB() __builtin_amdgcn_sched_barrier(0)

__global__ __launch_bounds__(512, 4) void fused_kernel(
    const u16* __restrict__ x, const u16* __restrict__ Are, const u16* __restrict__ Aim, const u16* __restrict__ Bre,
    const u16* __restrict__ Bim, const u16* __restrict__ Cre, const u16* __restrict__ Cim, const u16* __restrict__ logdt,
    const u16* __restrict__ Dsk, const u16* __restrict__ w1, const u16* __restrict__ b1, const u16* __restrict__ w2,
    u16* __restrict__ out, char* __restrict__ ws, int mask) {
    extern __shared__ __attribute__((aligned(16))) char smem[];
    const int tid = __builtin_amdgcn_workitem_id_x();
    const int l = tid & 63;
    const int w = __builtin_amdgcn_readfirstlane(tid >> 6);
    const int g = l >> 4, c16 = l & 15;
    const int bid = __builtin_amdgcn_workgroup_id_x() ^ SWAPR;
    const u32 lb = (u32)(size_t)(LDSP char*)smem;
    STAMP_DECL;
    STAMP(0);

    auto w2_load = [&](int ks, u32x4 (&wv)[5]) {
#pragma unroll
        for (int i = 0; i < 5; ++i) {
            const int n = i * 64 + l;
            const u32x4* pp = (const u32x4*)((const char*)w2 + (u32)((ks * 32 + n / 10) * 1280 + w * 160 + (n % 10) * 16));
            wv[i] = NT2 ? __builtin_nontemporal_load(pp) : *pp;
        }
    };
    auto w2_store = [&](int ks, const u32x4 (&wv)[5]) {
        char* tile = smem + w * 5632;
#pragma unroll
        for (int i = 0; i < 5; ++i) {
            const int n = i * 64 + l;
            *(u32x4*)(tile + (n / 10) * 176 + (n % 10) * 16) = wv[i];
        }
#pragma unroll
        for (int dd = 0; dd < 5; ++dd) {
            const char* a = tile + (16 * (g >> 1) + 4 * (g & 1) + (c16 >> 2)) * 176 + dd * 32 + 8 * (l & 3);
            const bf16x8 f = trfrag(a, a + 8 * 176);
            st128_sc1(ws, WS_W2T + (u32)(((w * 54 + ks) * 5 + dd) * 1024 + l * 16), __builtin_bit_cast(u32x4, f));
        }
    };

    const bool isG = bid >= 256;
    const int gb = bid & 255;
    const int xcd = gb & 7, idx = gb >> 3;
    const int tk = w >> 2, ch = (w >> 1) & 1, p = w & 1;
    const bool doconv = (mask & 2) && (isG ? (idx >= 28 && tk == 0) : (tk == 0 || idx >= 28));
    const int d0 = 80 * xcd + 2 * (isG ? idx + 8 : idx + 4 * tk);
    const int d = d0 + ch;
    u32 pa[6] = {0, 0, 0, 0, 0, 0}, pld = 0, pdk = 0;
    auto load_params = [&]() {
        const u32 pi = (u32)(d * NM + p * 64 + l);
        pa[0] = Are[pi]; pa[1] = Aim[pi]; pa[2] = Bre[pi]; pa[3] = Bim[pi]; pa[4] = Cre[pi]; pa[5] = Cim[pi];
        pld = logdt[d]; pdk = Dsk[d];
    };
    u32 tag = 0;
    bool pfd = false;
    const int t2 = 8 * (xcd >> 1) + (idx & 7), cg = 4 * (xcd & 1) + (idx >> 3);
    const bool has2 = (mask & 8) != 0;
    const int r32 = l & 31, pw = w & 3;
    const u32 gw2 = WS_W2T + (u32)(cg * 54 * 5120 + l * 16);
    const int i0 = ROT2 ? ((((idx & 7) * 27) >> 3) + (idx >> 3) * 7) % 27 : 0;

    if (isG) {
        __builtin_amdgcn_s_setprio(2);
        const bool has1 = (idx < 28) && (mask & 4);
        const int tt = 4 * (xcd >> 1) + (idx & 3);
        const int ct = 7 * (xcd & 1) + (idx >> 2);
        const int h = l >> 5;
        const bool half = (ct == 13);
        float* csum = (float*)(smem + 73728);

        const int th = pw & 1, fh = pw >> 1, tbe = w >> 2;
        const u32 stoff = WS_STAT + (u32)((tt * 128 + 64 * th + 32 * tbe + r32) * 8);
        auto epilogue1 = [&](const f32x16& a0, const f32x16& a1, u32x2 st0, bool pf) {
#pragma unroll 1
            for (int it = 0; it < SPIN_CAP; ++it) {
                if (wall(st0.y == tag)) break;
                asm volatile("global_load_dwordx2 %0, %1, %2 sc1\n\ts_waitcnt vmcnt(0)" : "=v"(st0) : "v"(stoff), "s"(ws) : "memory");
            }
            STAMP(3);
            const float* cp = csum + fh * 64 + 4 * h;
            const u32 abase = WS_ACT + (u32)(((tt * 8 + 4 * th + 2 * tbe + (r32 >> 4)) * 54 + ct * 4 + fh * 2) * 1024 + (h * 16 + c16) * 16);
            const float nmt = -lo16(st0.x), rt = 1.44269504f * __builtin_amdgcn_rcpf(hi16(st0.x));
            auto epi = [&](const f32x16& a, int c, int g0) {
#pragma unroll
                for (int s = 0; s < 2; ++s) {
                    u32 pk[4];
#pragma unroll
                    for (int mm = 0; mm < 2; ++mm) {
                        const int m = 2 * s + mm;
                        const f32x4 c4 = *(const f32x4*)(cp + c * 32 + 8 * m);
                        const f32x4 b4 = *(const f32x4*)(cp + 128 + c * 32 + 8 * m);
                        float r[4];
#pragma unroll
                        for (int i = 0; i < 4; ++i) {
                            const float h2 = __builtin_fmaf(__builtin_fmaf(nmt, c4[i], a[4 * m + i]), rt, b4[i]);
                            const float e = __builtin_amdgcn_exp2f(h2);
                            const float rr = __builtin_amdgcn_rcpf(__builtin_fmaf(e, e + 2.f, 2.f));
                            r[i] = h2 * __builtin_fmaf(-1.38629436f, rr, 0.69314718f);
                        }
                        pk[2 * mm] = pk2(r[0], r[1]);
                        pk[2 * mm + 1] = pk2(r[2], r[3]);
                    }
                    st128_sc1(ws, abase + c * 1024 + s * 512, u32x4{pk[0], pk[1], pk[2], pk[3]});
                }
            };
            epi(a0, th, 0);
            epi(a1, th ^ 1, 4);
            STAMP(4);
            WAITV(0);
            STAMP(5);
            if (l == 0) st32_sc1(ws, WS_ACTF + (u32)(((tt * 4 + 2 * th + tbe) * 32 + ct * 2 + fh) * 4), tag);
        };
        char* hand = smem + 32768 + pw * 2048 + l * 16;
        const bool eact = !(half && fh == 1);

        if (w >= 4) {
            const int lw = w - 4;
            const u32 ev = ld32_sc1(ws, WS_EPOCH);
            if (has1) {
                u32 gx[2], gw[2];
#pragma unroll
                for (int e = 0; e < 2; ++e) {
                    const int vw = 2 * lw + e;
                    gx[e] = (u32)((tt * 128 + 8 * vw + (l >> 3)) * 1280 + (((l & 7) ^ ((4 * e + g) & 7)) << 4));
                    int wch = c16 ^ ((g << 2) | (vw & 3));
                    if (half) wch &= 7;
                    gw[e] = (u32)((4 * vw + g) * 3456 + ct * 256 + (wch << 4));
                }
                const u32 ldst = lb + lw * 2048;
                const int j0 = ROT1 ? (3 * (idx >> 2) + ((5 * (idx & 3)) >> 1)) % 10 : 0;
                auto issueW = [&](auto NC) {
                    constexpr int N = decltype(NC)::value, j = N >> 1;
                    const int jr = (j + j0 >= 10) ? j + j0 - 10 : j + j0;
                    const u32 o = jr * (64 * 3456) + (N & 1) * (32 * 3456);
                    dma16<0>(w1, gw[0] + o, ldst + (N & 3) * 8192);
                    dma16<0>(w1, gw[1] + o, ldst + (N & 3) * 8192 + 1024);
                };
                auto issueX = [&](auto JC, auto UC) {
                    constexpr int j = decltype(JC)::value, U = decltype(UC)::value;
                    const int jr = (j + j0 >= 10) ? j + j0 - 10 : j + j0;
                    const u32 o = U * (64 * 1280) + jr * 128;
                    dma16<0>(x, gx[0] + o, ldst + 32768 + (j % 3) * 16384 + U * 8192);
                    dma16<0>(x, gx[1] + o, ldst + 32768 + (j % 3) * 16384 + U * 8192 + 1024);
                };
                issueW(IC<0>{}); issueX(IC<0>{}, IC<0>{}); issueX(IC<0>{}, IC<1>{}); issueW(IC<1>{}); issueW(IC<2>{});
                issueX(IC<1>{}, IC<0>{}); issueX(IC<1>{}, IC<1>{}); issueW(IC<3>{}); issueX(IC<2>{}, IC<0>{}); issueX(IC<2>{}, IC<1>{});
                u32 bv = 0, w2flag = 0;
                u32x2 st0;
                auto lstep = [&](auto NC) {
                    constexpr int N = decltype(NC)::value;
                    WAITBAR(2 * g1_allow(N) + (N >= 18 ? 3 : 0));
                    if constexpr (N >= 1 && N <= 16) issueW(IC<N + 3>{});
                    if constexpr (N >= 2 && N <= 15) issueX(IC<N / 2 + 2>{}, IC<N & 1>{});
                    if constexpr (N == 0) {
                        tag = mktag(take32<63>(ev));
                        if (tid == 256) st32_sc1(ws, WS_RD + bid * 4, tag);
                    }
                    if constexpr (N == 17) {
                        bv = ld16(b1, (u32)((ct * 128 + (tid & (half ? 63 : 127))) * 2));
                        st0 = ld64_sc1(ws, stoff);
                        w2flag = ld32_sc1(ws, WS_W2F + (u32)(cg * 256 + (l < 54 ? l : 0) * 4));
                    }
                };
                lstep(IC<0>{}); lstep(IC<1>{}); lstep(IC<2>{}); lstep(IC<3>{}); lstep(IC<4>{}); lstep(IC<5>{}); lstep(IC<6>{});
                lstep(IC<7>{}); lstep(IC<8>{}); lstep(IC<9>{}); lstep(IC<10>{}); lstep(IC<11>{}); lstep(IC<12>{}); lstep(IC<13>{});
                lstep(IC<14>{}); lstep(IC<15>{}); lstep(IC<16>{}); lstep(IC<17>{}); lstep(IC<18>{}); lstep(IC<19>{});
                asm volatile("s_waitcnt vmcnt(0)\n\ts_barrier" : "+v"(bv), "+v"(st0), "+v"(w2flag)::"memory");
                if (tid < 384) csum[128 + (tid & 127)] = bf2f(bv) * 1.44269504f;
                if (tid == 384) csum[256] = asf(tag);
                BAR();
                pfd = has2 && wall(w2flag == tag);
                if (eact) {
                    f32x16 a0, a1;
#pragma unroll
                    for (int i = 0; i < 4; ++i) {
                        const f32x4 u = *(const f32x4*)(hand + (i >> 1) * 8192 + (i & 1) * 1024);
                        const f32x4 v = *(const f32x4*)(hand + (2 + (i >> 1)) * 8192 + (i & 1) * 1024);
#pragma unroll
                        for (int e = 0; e < 4; ++e) { a0[4 * i + e] = u[e]; a1[4 * i + e] = v[e]; }
                    }
                    WAITL0();
                    epilogue1(a0, a1, st0, false);
                }
            } else {
                tag = mktag(take32<0>(ev));
            }
        } else if (has1) {
            const int xa = 32768 + th * 8192 + r32 * 128 + ((h ^ ((r32 >> 1) & 7)) << 4);
            int wa[2];
            {
                const int q = c16 >> 2, p = l & 3, blk = (l >> 4) & 1;
#pragma unroll
                for (int t = 0; t < 2; ++t) {
                    const int row = 8 * h + 4 * t + q;
                    const int f = (q << 2) | ((2 * h + t) & 3);
                    wa[t] = (row * 256 + (((2 * blk + (p >> 1)) ^ f) << 4) + 8 * (p & 1)) ^ (fh << 7) ^ (th << 6);
                }
            }
            f32x16 acc[2][2];
#pragma unroll
            for (int i = 0; i < 16; ++i) { acc[0][0][i] = 0.f; acc[0][1][i] = 0.f; acc[1][0][i] = 0.f; acc[1][1][i] = 0.f; }
            float cs = 0.f, cs2 = 0.f;
            u32x2 st0;
            const bf16x2 ones = __builtin_bit_cast(bf16x2, 0x3F803F80u);
            bf16x8 xf[2][2], wf[2][2];
            auto cstep = [&](auto MC) {
                constexpr int M = decltype(MC)::value, N = M >> 1, B = M & 1, P = B ^ 1;
                auto rdx = [&](int tb) { if constexpr (M < 40) xf[B][tb] = *(const bf16x8*)(smem + ((M >> 2) % 3) * 16384 + tb * 4096 + (xa ^ ((M & 3) << 5))); };
                auto rdw = [&](int c) {
                    if constexpr (M < 40) wf[B][c] = trfrag(smem + (N & 3) * 8192 + B * 4096 + (wa[0] ^ (c << 6)), smem + (N & 3) * 8192 + B * 4096 + (wa[1] ^ (c << 6)));
                };
                SB();
                if constexpr (B == 0) BAR();
                if constexpr (M == 0) STAMP(1);
                if constexpr (M >= 1) acc[0][0] = mfma32(wf[P][0], xf[P][0], acc[0][0]);
                SB();
                rdx(0); rdw(0);
                SB();
                if constexpr (M >= 1) acc[1][0] = mfma32(wf[P][0], xf[P][1], acc[1][0]);
                SB();
                rdx(1); rdw(1);
                SB();
                if constexpr (M >= 1) acc[0][1] = mfma32(wf[P][1], xf[P][0], acc[0][1]);
                SB();
                if constexpr (M >= 1) {
                    const u32x4 wv = __builtin_bit_cast(u32x4, wf[P][0]);
                    const u32 a0 = wv[0], a1 = wv[1], a2_ = wv[2], a3 = wv[3];
                    cs = __builtin_amdgcn_fdot2_f32_bf16(__builtin_bit_cast(bf16x2, a0), ones, cs, false);
                    cs2 = __builtin_amdgcn_fdot2_f32_bf16(__builtin_bit_cast(bf16x2, a1), ones, cs2, false);
                    cs = __builtin_amdgcn_fdot2_f32_bf16(__builtin_bit_cast(bf16x2, a2_), ones, cs, false);
                    cs2 = __builtin_amdgcn_fdot2_f32_bf16(__builtin_bit_cast(bf16x2, a3), ones, cs2, false);
                }
                if constexpr (M == 36) st0 = ld64_sc1(ws, stoff);
                SB();
                if constexpr (M >= 1) acc[1][1] = mfma32(wf[P][1], xf[P][1], acc[1][1]);
            };
#define C4(m) cstep(IC<m>{}); cstep(IC<m + 1>{}); cstep(IC<m + 2>{}); cstep(IC<m + 3>{});
            C4(0) C4(4) C4(8) C4(12) C4(16) C4(20) C4(24) C4(28) C4(32) C4(36)
#undef C4
            cstep(IC<40>{});
            STAMP(2);
            cs += cs2;
            cs += asf((u32)__builtin_amdgcn_ds_bpermute((l ^ 32) * 4, (int)asu(cs)));
            if (l < 32) csum[fh * 64 + th * 32 + l] = cs;
#pragma unroll
            for (int i = 0; i < 4; ++i) {
                *(f32x4*)(hand + (i >> 1) * 8192 + (i & 1) * 1024) = f32x4{acc[1][0][4 * i], acc[1][0][4 * i + 1], acc[1][0][4 * i + 2], acc[1][0][4 * i + 3]};
                *(f32x4*)(hand + (2 + (i >> 1)) * 8192 + (i & 1) * 1024) = f32x4{acc[1][1][4 * i], acc[1][1][4 * i + 1], acc[1][1][4 * i + 2], acc[1][1][4 * i + 3]};
            }
            asm volatile("s_waitcnt vmcnt(0) lgkmcnt(0)\n\ts_barrier" : "+v"(st0)::"memory");
            tag = asu(csum[256]);
            if (eact) epilogue1(acc[0][0], acc[0][1], st0, false);
        }
        if (idx >= 28) {
            if (w < 4) { const u32 ev = ld32_sc1(ws, WS_EPOCH); tag = mktag(take32<0>(ev)); }
            if (doconv) load_params();
            if (mask & 1) {
                const int k0 = gb - 224;
                u32x4 wv[5], wv2[5];
                w2_load(k0, wv);
                if (k0 < 22) w2_load(k0 + 32, wv2);
                w2_store(k0, wv);
                if (k0 < 22) w2_store(k0 + 32, wv2);
                WAITV(0);
                if (l == 0) st32_sc1(ws, WS_W2F + (u32)((w * 64 + k0) * 4), tag);
                if (k0 < 22 && l == 0) st32_sc1(ws, WS_W2F + (u32)((w * 64 + k0 + 32) * 4), tag);
            }
            BAR();
            if (tid == 0) st32_sc1(ws, WS_RD + bid * 4, tag);
        } else if (!has1 && w < 4) {
            const u32 ev = ld32_sc1(ws, WS_EPOCH);
            tag = mktag(take32<0>(ev));
        }

    } else {
    const int cb = bid;
    const int tg = 64 * (xcd >> 1) + 32 * (xcd & 1) + idx;
    u32 xv[5];
    if (mask & 1) {
        const u32* xr = (const u32*)x + (u32)((tg * 8 + w) * 320 + l);
#pragma unroll
        for (int i = 0; i < 5; ++i) xv[i] = xr[64 * i];
    }
    const u32 ev = ld32_sc1(ws, WS_EPOCH);
    if (doconv) load_params();
    char* lnt = smem + 65536;
    float mb = 0.f, den = 1.f;
    if (mask & 1) {
        float s = 0.f;
#pragma unroll
        for (int i = 0; i < 5; ++i) s += lo16(xv[i]) + hi16(xv[i]);
        const float mean = wave_sum(s) * (1.f / 640.f);
        float q = 0.f;
#pragma unroll
        for (int i = 0; i < 5; ++i) {
            const float a = lo16(xv[i]) - mean, b = hi16(xv[i]) - mean;
            q = __builtin_fmaf(a, a, q); q = __builtin_fmaf(b, b, q);
        }
        const float var = wave_sum(q) * (1.f / 640.f);
        mb = rbf(mean);
        den = rbf(__builtin_sqrtf(rbf(rbf(var) + 1e-5f)));
        const float rden = 1.0f / den;
        char* lw = lnt + w * 1280 + l * 4 + w * 64;
#pragma unroll
        for (int i = 0; i < 5; ++i) {
            const u32 dd = pk2(lo16(xv[i]) - mb, hi16(xv[i]) - mb);
            const u32 o = pk2(lo16(dd) * rden, hi16(dd) * rden);
            if (i < 3) *(u32*)(lw + 256 * i) = o;
            else *(u32*)(lw + 256 * i - ((256 * i + l * 4 + w * 64 >= 1280) ? 1280 : 0)) = o;
        }
    }
    tag = mktag(doconv ? take32<8>(ev) : take32<0>(ev));
    STAMP(1);
    if ((mask & 1) && l == 0) st64_sc1(ws, WS_STAT + (u32)((tg * 8 + w) * 8), u32x2{pk2(mb, den), tag});
    BAR();
    if (tid == 0) st32_sc1(ws, WS_RD + bid * 4, tag);
    if (mask & 1) {
        const int q = c16 >> 2;
        auto piece = [&](int i) {
            int p0 = 2 * (i * 4 + g) + 4 * q, p1 = p0 + 16;
            if (p0 >= 80) p0 -= 80;
            if (p1 >= 80) p1 -= 80;
            const bf16x8 f = trfrag(lnt + q * 1280 + p0 * 16 + 8 * (l & 3), lnt + (q + 4) * 1280 + p1 * 16 + 8 * (l & 3));
            st128_sc1(ws, WS_XB + (u32)(tg * 10240 + i * 1024 + l * 16), __builtin_bit_cast(u32x4, f));
        };
        piece(w);
        if (w >= 6) piece(2 + w);
    }
    STAMP(2);
    if (!doconv) {
        WAITBAR(0);
        if (!(mask & 2) && (mask & 1) && tid == 0) st32_sc1(ws, WS_XBF + cb * 4, tag);
        if (cb == 0 && w == 7) {
            poll128(ws, WS_RD + l * 16, tag);
            poll128(ws, WS_RD + 1024 + l * 16, tag);
            if (l == 0) st32_sc1(ws, WS_EPOCH, tag);
        }
        return;
    }
    }
    if (doconv) {
        const float dt = rbf(__builtin_expf(bf2f(pld)));
        const float lr2 = (dt * bf2f(pa[0])) * 1.44269504f;
        const float li = dt * bf2f(pa[1]);
        const float rh = li * 0.15915494f;
        const float rl = __builtin_fmaf(li, 0.15915494f, -rh) + li * 3.0908620e-9f;
        auto E = [&](float t) {
            const float ph = rh * t;
            const float er = __builtin_fmaf(rl, t, __builtin_fmaf(rh, t, -ph));
            const float fr = (ph - __builtin_rintf(ph)) + er;
            const float m = __builtin_amdgcn_exp2f(lr2 * t);
            return Cplx{m * __builtin_amdgcn_cosf(fr), m * __builtin_amdgcn_sinf(fr)};
        };
        char* scr = smem + w * 4608;
        auto put = [&](int row, Cplx v) { *(u32*)(scr + row * 288 + l * 4) = pkh(v.r, v.i); };
        const char* frd = scr + c16 * 288 + g * 16;
        f16x8 bq[4];
        float inv_scale;
        {
            Cplx c = cmul(Cplx{bf2f(pa[4]), bf2f(pa[5])}, Cplx{dt * bf2f(pa[2]), dt * bf2f(pa[3])});
            const float mx = wave_max(__builtin_fmaxf(__builtin_fabsf(c.r), __builtin_fabsf(c.i)));
            int ex = 14 - __builtin_amdgcn_frexp_expf(mx);
            ex = ex > 100 ? 100 : (ex < -100 ? -100 : ex);
            inv_scale = __builtin_amdgcn_ldexpf(1.f, -ex);
            c.r = __builtin_amdgcn_ldexpf(c.r, ex);
            c.i = -__builtin_amdgcn_ldexpf(c.i, ex);
            Cplx e8 = E(8.f);
            e8.i = -e8.i;
            const Cplx e16 = csq(e8), e32 = csq(e16), e64 = csq(e32);
            Cplx b[8];
            b[0] = c; b[1] = cmul(c, e8); b[2] = cmul(c, e16); b[3] = cmul(b[1], e16);
#pragma unroll
            for (int j = 0; j < 4; ++j) b[4 + j] = cmul(b[j], e32);
#pragma unroll
            for (int j = 0; j < 8; ++j) { put(j, b[j]); put(8 + j, cmul(b[j], e64)); }
#pragma unroll
            for (int s = 0; s < 4; ++s) bq[s] = *(const f16x8*)(frd + s * 64);
        }
        WAITBAR(0);
        if (tid == 0 && (mask & 1) && !isG) st32_sc1(ws, WS_XBF + gb * 4, tag);
        u32 ix[9];
#pragma unroll
        for (int k = 0; k < 9; ++k) ix[k] = g_idx.v[k * 128 + p * 64 + l];
        auto tile_mm = [&]() {
            f32x4 c = {0, 0, 0, 0};
#pragma unroll
            for (int s = 0; s < 4; ++s) c = mfmah(*(const f16x8*)(frd + s * 64), bq[s], c);
            return c;
        };
        f32x4 T0, T1, T2;
        {
            const Cplx e1 = E(1.f), e2 = csq(e1), e3 = cmul(e2, e1), e4 = csq(e2), e5 = cmul(e4, e1), e6 = cmul(e4, e2), e7 = cmul(e4, e3);
            const Cplx e128 = E(128.f);
            put(0, Cplx{1.f, 0.f}); put(1, e1); put(2, e2); put(3, e3); put(4, e4); put(5, e5); put(6, e6); put(7, e7);
            put(8, e128); put(9, cmul(e128, e1)); put(10, cmul(e128, e2)); put(11, cmul(e128, e3));
            put(12, cmul(e128, e4)); put(13, cmul(e128, e5)); put(14, cmul(e128, e6)); put(15, cmul(e128, e7));
            T0 = tile_mm();
            const Cplx e256 = csq(e128), e384 = cmul(e256, e128), e512 = csq(e256);
            const Cplx e640 = cmul(e512, e128), e768 = cmul(e512, e256), e896 = cmul(e512, e384);
            put(0, e256); put(1, cmul(e256, e2)); put(2, cmul(e256, e4)); put(3, cmul(e256, e6));
            put(4, e384); put(5, cmul(e384, e2)); put(6, cmul(e384, e4)); put(7, cmul(e384, e6));
            put(8, e512); put(9, cmul(e512, e4)); put(10, e640); put(11, cmul(e640, e4));
            put(12, e768); put(13, cmul(e768, e4)); put(14, e896); put(15, cmul(e896, e4));
            T1 = tile_mm();
            const Cplx e1k = E(1024.f);
            put(0, e1k); put(1, cmul(e1k, e128)); put(2, cmul(e1k, e256)); put(3, cmul(e1k, e384)); put(4, cmul(e1k, e512));
            put(5, cmul(e1k, e640)); put(6, cmul(e1k, e768)); put(7, cmul(e1k, e896)); put(8, csq(e1k));
            T2 = tile_mm();
        }
        STAMP(3);
        {
            float* tabp = (float*)scr;
            const int j = c16;
            *(f32x4*)(tabp + 128 * (g >> 1) + 4 * (g & 1) + 8 * j) = T0 * inv_scale;
            T1 *= inv_scale;
            T2 *= inv_scale;
            if (g < 2) {
                *(f32x4*)(tabp + 256 + 64 * g + 4 * j) = T1;
#pragma unroll
                for (int i = 0; i < 4; ++i) tabp[512 + 16 * (4 * g + i) + j] = T2[i];
            } else {
                *(f32x2*)(tabp + 384 + 64 * (g - 2) + 2 * j) = f32x2{T1[0], T1[1]};
                *(f32x2*)(tabp + 384 + 64 * (g - 2) + 32 + 2 * j) = f32x2{T1[2], T1[3]};
                if (g == 2) tabp[640 + j] = T2[0];
            }
        }
        if (w == 0) poll128(ws, WS_XBF + l * 16, tag);
        BAR();
        STAMP(4);
        char* U = smem + 36864 + tk * 9216;
#pragma unroll
        for (int i = 0; i < 2; ++i) {
            const int c = ((w & 3) * 2 + i) * 32 + (l >> 1);
            const int chs = (l & 1) ^ ((c >> 3) & 1);
            dma16<1>(ws, WS_XB + (u32)(c * 10240 + (d0 + chs) * 16), lb + 36864 + tk * 9216 + 1024 + ((w & 3) * 2 + i) * 1024);
        }
        if ((w & 3) == 0) *(u32x4*)(U + l * 16) = u32x4{0, 0, 0, 0};
        char* tab = smem + 76288 + (w >> 1) * 1344;
        {
            const float* t0 = (const float*)(smem + (w & 6) * 4608);
            const float* t1 = (const float*)(smem + (w | 1) * 4608);
#pragma unroll
            for (int it = 0; it < 3; ++it) {
                const int i = it * 128 + p * 64 + l;
                if (i < 328) {
                    const f32x2 a = *(const f32x2*)(t0 + 2 * i), b = *(const f32x2*)(t1 + 2 * i);
                    *(u32*)(tab + 4 * i) = pk2(a.x + b.x, a.y + b.y);
                } else if (i == 330) *(u32*)(tab + 4 * i) = 0u;
            }
        }
        BAR();
        STAMP(5);
        char* KE = smem + (w >> 1) * 8448;
        char* KO = KE + 4160;
        {
            const int m = p * 64 + l;
            u32 e[9];
#pragma unroll
            for (int k = 0; k < 9; ++k) {
                u16x2 v;
                v.x = *(const u16*)(tab + (ix[k] & 0xFFFFu));
                v.y = *(const u16*)(tab + (ix[k] >> 16));
                e[k] = __builtin_bit_cast(u32, v);
            }
            *(u32x4*)(KE + m * 32) = u32x4{e[0], e[1], e[2], e[3]};
            *(u32x4*)(KE + m * 32 + 16) = u32x4{e[4], e[5], e[6], e[7]};
            u32 o[8];
#pragma unroll
            for (int k = 0; k < 8; ++k) o[k] = __builtin_amdgcn_alignbit(e[k + 1], e[k], 16);
            *(u32x4*)(KO + m * 32) = u32x4{o[0], o[1], o[2], o[3]};
            *(u32x4*)(KO + m * 32 + 16) = u32x4{o[4], o[5], o[6], o[7]};
            if (p == 0 && l < 4) *(u32x4*)(KE + 4096 + (l & 1) * 16 + (l >> 1) * 4160) = u32x4{0, 0, 0, 0};
        }
        WAITBAR(0);
        STAMP(6);
        f32x4 y[8];
#pragma unroll
        for (int a = 0; a < 8; ++a) y[a] = f32x4{0, 0, 0, 0};
#pragma unroll 1
        for (int rr = 0; rr < 4; ++rr) {
            const int r = 8 * p + 2 * rr;
            const int c = 2 * (c16 - r - 1) + g + 32;
            const char* ub = U + c * 32 + ((ch ^ ((c >> 3) & 1)) << 4);
            bf16x8 B[8];
#pragma unroll
            for (int a = 0; a < 8; ++a) B[a] = *(const bf16x8*)(ub + a * 1024);
            const int i0 = 2031 - 16 * r - c16 + 8 * g;
            const char* kb = ((i0 & 1) ? KO : KE) + (i0 >> 1) * 4;
#pragma unroll
            for (int q = 0; q < 8; ++q) {
                const u32x4a av = *(const u32x4a*)(kb - q * 512);
                const bf16x8 A = __builtin_bit_cast(bf16x8, av);
#pragma unroll
                for (int a = q; a < 8; ++a) y[a] = mfma(A, B[a - q], y[a]);
            }
        }
        STAMP(7);
        const float dsk = bf2f(pdk);
        BAR();
        char* ew = smem + w * 4096 + l * 16;
        const char* eo = smem + (w ^ 1) * 4096 + l * 16;
        char* so = smem + 55296 + w * 2048;
        auto fin = [&](int a, f32x4 yy) {
            yy += *(const f32x4*)(eo + a * 1024);
            const int cu = 2 * (16 * (4 * p + a) + c16) + (g >> 1) + 32;
            const u32x2 uv = *(const u32x2*)(U + cu * 32 + ((ch ^ ((cu >> 3) & 1)) << 4) + (g & 1) * 8);
            *(u32x2*)(so + a * 512 + c16 * 32 + g * 8) =
                u32x2{pk2(__builtin_fmaf(lo16(uv.x), dsk, yy[0]), __builtin_fmaf(hi16(uv.x), dsk, yy[1])),
                      pk2(__builtin_fmaf(lo16(uv.y), dsk, yy[2]), __builtin_fmaf(hi16(uv.y), dsk, yy[3]))};
        };
        if (p == 0) {
#pragma unroll
            for (int a = 0; a < 4; ++a) *(f32x4*)(ew + a * 1024) = y[4 + a];
            BAR();
#pragma unroll
            for (int a = 0; a < 4; ++a) fin(a, y[a]);
        } else {
#pragma unroll
            for (int a = 0; a < 4; ++a) *(f32x4*)(ew + a * 1024) = y[a];
            BAR();
#pragma unroll
            for (int a = 0; a < 4; ++a) fin(a, y[4 + a]);
        }
#pragma unroll
        for (int i = 0; i < 2; ++i) {
            const u32x4 v = *(const u32x4*)(so + i * 1024 + l * 16);
            st128_sc1(ws, WS_ST + (u32)(d * 4096 + p * 2048 + i * 1024 + l * 16), v);
        }
        STAMP(8);
        WAITV(0);
        if (l == 0) st32_sc1(ws, WS_SF + (u32)((p * 640 + d) * 4), tag);
        STAMP(9);
        if (!isG) STAMP_FLUSH();
    } else if (isG && idx >= 28 && (mask & 2)) {
        BAR(); BAR(); BAR(); BAR(); BAR(); BAR();
    }
    if (!isG) return;
    {
        auto rot = [&](int I) { return (I + i0 >= 27) ? I + i0 - 27 : I + i0; };
        auto pf_w2 = [&](int m) {
#pragma unroll
            for (int e = 0; e < 2; ++e) {
                int f = 10 * i0 + 8 * m + 2 * pw + e;
                if (f >= 270) f -= 270;
                dma16<1>(ws, gw2 + f * 1024, lb + m * 8192 + (2 * pw + e) * 1024);
            }
        };
        if (has2) {
            const int tbk = w & 3, kp = w >> 2;
            BAR();
            if (w >= 4) {
                if (!pfd) poll32(ws, WS_W2F + (u32)(cg * 256 + (l < 54 ? l : 0) * 4), tag);
#pragma unroll
                for (int m = 0; m < 8; ++m) pf_w2(m);
            }
            int fsrc = 10 * i0 + w + 64;
            if (fsrc >= 270) fsrc -= 270;
            auto issue_w2 = [&](auto MC) {
                constexpr int M = decltype(MC)::value;
                if (M < 33 || w < 6) dma16<1>(ws, gw2 + fsrc * 1024, lb + (M & 7) * 8192 + w * 1024);
                else dma16<1>(ws, gw2, lb + 73728 + 2048 + (w & 1) * 1024);
                fsrc += 8;
                if (fsrc >= 270) fsrc -= 270;
            };
            STAMP(6);
            u32 rmask = 0;
            const u32 afo = WS_ACTF + (u32)(((t2 * 2 + (tbk >> 1)) * 32 + r32) * 4);
            auto need = [&](int bit) {
                if (__builtin_expect(!((rmask >> bit) & 1u), 0)) {
#pragma unroll 1
                    for (int it = 0; it < SPIN_CAP; ++it) {
                        u32 v;
                        asm volatile("global_load_dword %0, %1, %2 sc1\n\ts_waitcnt vmcnt(0)" : "=v"(v) : "v"(afo), "s"(ws) : "memory");
                        rmask = (u32)__builtin_amdgcn_ballot_w64(v == tag);
                        if ((rmask >> bit) & 1u) break;
                        __builtin_amdgcn_s_sleep(PSL1);
                    }
                }
            };
            const u32 ga = WS_ACT + (u32)(((4 * t2 + tbk) * 54 + kp) * 1024 + l * 16);
            constexpr int AD = 6;
            u32x4 af[8];
#pragma unroll
            for (int i = 0; i < AD; ++i) { need(rot(i)); af[i] = ld128_sc1(ws, ga + rot(i) * 2048); }
            f32x4 a2[5];
#pragma unroll
            for (int d = 0; d < 5; ++d) a2[d] = f32x4{0, 0, 0, 0};
            const int wl = kp * 5120 + l * 16;
            u32 sf0 = 0, sf1 = 0;
            bool sdone = false;
            const int n0 = w * 64 + l, n1 = 512 + (w & 1) * 64 + l;
            const u32 gx0 = (u32)((t2 * 64 + n0 / 10) * 1280 + cg * 160 + (n0 % 10) * 16);
            const u32 gx1 = (u32)((t2 * 64 + n1 / 10) * 1280 + cg * 160 + (n1 % 10) * 16);
            const u32 gs0 = WS_ST + (u32)((cg * 80 + (n0 >> 3)) * 4096 + t2 * 128 + (n0 & 7) * 16);
            const u32 gs1 = WS_ST + (u32)((cg * 80 + (n1 >> 3)) * 4096 + t2 * 128 + (n1 & 7) * 16);
            constexpr int XS = 16384, SS = 65536;
            const u32 sfp = WS_SF + (u32)(((t2 >> 4) * 640 + cg * 80) * 4);
            bf16x8 wq[2][5];
            auto load_s = [&]() {
                dma16<1>(ws, gs0, lb + SS + w * 1024);
                if (w < 2) dma16<1>(ws, gs1, lb + SS + 8192 + w * 1024);
            };
            auto st2 = [&](auto IC_) {
                constexpr int I = decltype(IC_)::value, B = I & 1;
                constexpr int allow = g2_allow(I, AD);
                SB();
                if constexpr (I == 24)
                    asm volatile("s_waitcnt vmcnt(%3)\n\ts_barrier" : "+v"(af[I & 7]), "+v"(sf0), "+v"(sf1) : "n"(allow < g2_ops(21, AD) + g2_ops(22, AD) + g2_ops(23, AD) ? allow : g2_ops(21, AD) + g2_ops(22, AD) + g2_ops(23, AD)) : "memory");
                else
                    asm volatile("s_waitcnt vmcnt(%1)\n\ts_barrier" : "+v"(af[I & 7]) : "n"(allow) : "memory");
                const bf16x8 bfrag = __builtin_bit_cast(bf16x8, af[(I + 7) & 7]);
#pragma unroll
                for (int d = 0; d < 5; ++d) {
                    if constexpr (I >= 1) a2[d] = mfma(wq[B ^ 1][d], bfrag, a2[d]);
                    SB();
                    {
                        const int o0 = ((10 * I + d) & 63) * 1024;
                        if (o0 + 5120 + 1024 <= 65536) wq[B][d] = *(const bf16x8*)(smem + wl + o0);
                        else wq[B][d] = *(const bf16x8*)(smem + ((wl + o0) & 65535));
                    }
                    if (d == 0) { if constexpr (g2_lo(I) < g2_hi(I)) issue_w2(IC<g2_lo(I)>{}); }
                    if (d == 1) { if constexpr (g2_lo(I) + 1 < g2_hi(I)) issue_w2(IC<g2_lo(I) + 1>{}); }
                    if (d == 2) { if constexpr (I + AD < 27) { need(rot(I + AD)); af[(I + AD) & 7] = ld128_sc1(ws, ga + rot(I + AD) * 2048); } }
                    SB();
                }
                if constexpr (I == 20) {
                    sf0 = ld32_sc1(ws, sfp + l * 4);
                    sf1 = ld32_sc1(ws, sfp + 256 + c16 * 4);
                }
                if constexpr (I == 23) {
                    dma16<0>(x, gx0, lb + XS + w * 1024);
                    if (w < 2) dma16<0>(x, gx1, lb + XS + 8192 + w * 1024);
                }
                if constexpr (I == 24) {
                    sdone = wall(sf0 == tag && sf1 == tag);
                    if (sdone) load_s();
                }
            };
            st2(IC<0>{}); st2(IC<1>{}); st2(IC<2>{}); st2(IC<3>{}); st2(IC<4>{}); st2(IC<5>{}); st2(IC<6>{}); st2(IC<7>{}); st2(IC<8>{});
            st2(IC<9>{}); st2(IC<10>{}); st2(IC<11>{}); st2(IC<12>{}); st2(IC<13>{}); st2(IC<14>{}); st2(IC<15>{}); st2(IC<16>{});
            st2(IC<17>{}); st2(IC<18>{}); st2(IC<19>{}); st2(IC<20>{}); st2(IC<21>{}); st2(IC<22>{}); st2(IC<23>{}); st2(IC<24>{});
            st2(IC<25>{}); st2(IC<26>{});
            {
                const bf16x8 bfrag = __builtin_bit_cast(bf16x8, af[26 & 7]);
#pragma unroll
                for (int d = 0; d < 5; ++d) a2[d] = mfma(wq[0][d], bfrag, a2[d]);
            }
            STAMP(7);
            if (!sdone) {
                poll32(ws, sfp + l * 4, tag);
                poll32(ws, sfp + 256 + c16 * 4, tag);
                load_s();
            }
            char* ex = smem + 30720 + tbk * 5120 + l * 16;
            if (kp == 0) { *(f32x4*)(ex + 3072) = a2[3]; *(f32x4*)(ex + 4096) = a2[4]; }
            else { *(f32x4*)(ex) = a2[0]; *(f32x4*)(ex + 1024) = a2[1]; *(f32x4*)(ex + 2048) = a2[2]; }
            WAITBAR(0);
            STAMP(8);
            auto fin = [&](int d, f32x4 o) {
                o += *(const f32x4*)(ex + d * 1024);
                char* xp = smem + XS + (16 * tbk + c16) * 160 + d * 32 + 8 * g;
                const u32x2 xv = *(const u32x2*)xp;
                const s16x4 sv4 = __builtin_amdgcn_ds_read_tr16_b64_v4i16(
                    (LDSP s16x4*)(smem + SS + (16 * d + 4 * g + (c16 >> 2)) * 128 + tbk * 32 + 8 * (l & 3)));
                const u32x2 sv = __builtin_bit_cast(u32x2, sv4);
                const float xs[4] = {lo16(xv.x), hi16(xv.x), lo16(xv.y), hi16(xv.y)};
                const float ss[4] = {lo16(sv.x), hi16(sv.x), lo16(sv.y), hi16(sv.y)};
                float r[4];
#pragma unroll
                for (int i = 0; i < 4; ++i) {
                    const float sg = __builtin_amdgcn_rcpf(1.f + __builtin_amdgcn_exp2f(-1.44269504f * o[i]));
                    r[i] = __builtin_fmaf(ss[i] * o[i], sg, xs[i]);
                }
                *(u32x2*)xp = u32x2{pk2(r[0], r[1]), pk2(r[2], r[3])};
            };
            if (kp == 0) { fin(0, a2[0]); fin(1, a2[1]); fin(2, a2[2]); }
            else { fin(3, a2[3]); fin(4, a2[4]); }
            BAR();
            *(u32x4*)((char*)out + gx0) = *(const u32x4*)(smem + XS + n0 * 16);
            if (w < 2) *(u32x4*)((char*)out + gx1) = *(const u32x4*)(smem + XS + n1 * 16);
        }
        STAMP(9);
        if (mask & 12) STAMP_FLUSH();
    }
}
#if defined(__HIP_DEVICE_COMPILE__)
#pragma clang attribute pop
#endif

extern "C" void kernel_launch(void* const* d_in, const int* in_sizes, int n_in, void* d_out, int out_size, void* d_ws,
                              size_t ws_size, hipStream_t stream) {
    (void)hipFuncSetAttribute((const void*)fused_kernel, hipFuncAttributeMaxDynamicSharedMemorySize, LDS_BYTES);
#define A(i) (const u16*)d_in[i]
    fused_kernel<<<512, 512, LDS_BYTES, stream>>>(A(0), A(1), A(2), A(3), A(4), A(5), A(6), A(7), A(8), A(10), A(11), A(12),
                                                  (u16*)d_out, (char*)d_ws, 15);
}
```

```cpp
#include <hip/hip_runtime.h>
#include <stdint.h>
#include <type_traits>
#include <initializer_list>

typedef unsigned short u16;
typedef unsigned int u32;
typedef unsigned long long u64;
typedef __attribute__((ext_vector_type(8))) __bf16 bf16x8;
typedef __attribute__((ext_vector_type(2))) __bf16 bf16x2;
typedef __attribute__((ext_vector_type(8))) _Float16 f16x8;
typedef __attribute__((ext_vector_type(2))) _Float16 f16x2;
typedef __attribute__((ext_vector_type(4))) short s16x4;
typedef __attribute__((ext_vector_type(2))) u16 u16x2;
typedef __attribute__((ext_vector_type(4))) float f32x4;
typedef __attribute__((ext_vector_type(16))) float f32x16;
typedef __attribute__((ext_vector_type(2))) float f32x2;
typedef __attribute__((ext_vector_type(4))) u32 u32x4;
typedef __attribute__((ext_vector_type(2))) u32 u32x2;
typedef __attribute__((ext_vector_type(4), aligned(4))) u32 u32x4a;
#define LDSP __attribute__((address_space(3)))
#define DI __device__ __forceinline__
template <int N> using IC = std::integral_constant<int, N>;

#define MULTI 0

#define WARM 0
#define TLBP 0
#define DELAY 0
#define NT1 0
#define NT2 0
#define W2DELAY 0
#define PSL1 8
#define PSL2 16
#define CPRIO 0
#define SWAPR 0
#define ROT1 0
#define ROT2 0
#define CWARM 5
constexpr int NM = 128;
constexpr u32 WS_EPOCH = 0, WS_RD = 1024, WS_XBF = 4096, WS_W2F = 5120, WS_SF = 8192, WS_ACTF = 16384, WS_STAT = 32768;
constexpr u32 WS_XB = 1u << 20, WS_ST = 4u << 20, WS_W2T = 8u << 20, WS_ACT = 12u << 20;
constexpr int LDS_BYTES = 81920;
constexpr int SPIN_CAP = 1 << 16;

struct IdxTab {
    u32 v[9 * 128];
    static constexpr int idx(int l) {
        if (l < 0) return 660;
        if (l < 256) return l;
        int sh = l < 512 ? 1 : (l < 1024 ? 2 : 3);
        int q = (l + ((1 << (sh - 1)) - 1) + ((l >> sh) & 1)) >> sh;
        return 128 * sh + q;
    }
    constexpr IdxTab() : v() {
        for (int k = 0; k < 9; ++k)
            for (int m = 0; m < 128; ++m)
                v[k * 128 + m] = (u32)(2 * idx(2047 - (16 * m + 2 * k))) | ((u32)(2 * idx(2047 - (16 * m + 2 * k + 1))) << 16);
    }
};
__device__ const IdxTab g_idx = IdxTab();

#if defined(__HIP_DEVICE_COMPILE__)
#pragma clang attribute push(__attribute__((target("no-packed-fp32-ops"))), apply_to = function)
#endif
DI float asf(u32 v) { return __builtin_bit_cast(float, v); }
DI u32 asu(float v) { return __builtin_bit_cast(u32, v); }
DI bool wall(bool p) { return __builtin_amdgcn_ballot_w64(p) == ~0ull; }
DI float bf2f(u32 v) { return asf(v << 16); }
DI u32 pk2(float a, float b) { f32x2 f = {a, b}; return __builtin_bit_cast(u32, __builtin_convertvector(f, bf16x2)); }
DI u32 pkh(float a, float b) { f32x2 f = {a, b}; return __builtin_bit_cast(u32, __builtin_convertvector(f, f16x2)); }
DI float rbf(float a) { return asf(pk2(a, a) << 16); }
DI float lo16(u32 v) { return asf(v << 16); }
DI float hi16(u32 v) { return asf(v & 0xFFFF0000u); }
DI u32 mktag(u32 e) { return e * 0x9E3779B1u + 0x7F4A7C15u; }
template <int CTRL, int RM>
DI float dpp(float v) { return asf((u32)__builtin_amdgcn_update_dpp(0, (int)asu(v), CTRL, RM, 0xf, true)); }
DI float wave_sum(float v) {
    v += dpp<0x111, 0xf>(v); v += dpp<0x112, 0xf>(v); v += dpp<0x114, 0xf>(v); v += dpp<0x118, 0xf>(v);
    v += dpp<0x142, 0xa>(v); v += dpp<0x143, 0xc>(v);
    return asf((u32)__builtin_amdgcn_readlane((int)asu(v), 63));
}
DI float wave_max(float v) {
    v = __builtin_fmaxf(v, dpp<0x111, 0xf>(v)); v = __builtin_fmaxf(v, dpp<0x112, 0xf>(v));
    v = __builtin_fmaxf(v, dpp<0x114, 0xf>(v)); v = __builtin_fmaxf(v, dpp<0x118, 0xf>(v));
    v = __builtin_fmaxf(v, dpp<0x142, 0xa>(v)); v = __builtin_fmaxf(v, dpp<0x143, 0xc>(v));
    return asf((u32)__builtin_amdgcn_readlane((int)asu(v), 63));
}

#define WAITV(n) asm volatile("s_waitcnt vmcnt(%0)" ::"n"(n) : "memory")
#define WAITL0() asm volatile("s_waitcnt lgkmcnt(0)" ::: "memory")
#define BAR() asm volatile("s_waitcnt lgkmcnt(0)\n\ts_barrier" ::: "memory")
#define WAITBAR(n) asm volatile("s_waitcnt vmcnt(%0) lgkmcnt(0)\n\ts_barrier" ::"n"(n) : "memory")

#define STAMP(i)
#define STAMP_DECL
#define STAMP_FLUSH()

#define NOSC1 0
template <int SC1>
DI void dma16(const void* base, u32 voff, u32 lds) {
    if constexpr (SC1 && !NOSC1) asm volatile("s_mov_b32 m0, %2\n\ts_nop 0\n\tglobal_load_lds_dwordx4 %0, %1 sc1" ::"v"(voff), "s"(base), "s"(lds) : "memory");
    else if constexpr (NT1) asm volatile("s_mov_b32 m0, %2\n\ts_nop 0\n\tglobal_load_lds_dwordx4 %0, %1 nt" ::"v"(voff), "s"(base), "s"(lds) : "memory");
    else asm volatile("s_mov_b32 m0, %2\n\ts_nop 0\n\tglobal_load_lds_dwordx4 %0, %1" ::"v"(voff), "s"(base), "s"(lds) : "memory");
}
DI u32 ld32_sc1(const void* b, u32 o) { u32 v; asm volatile("global_load_dword %0, %1, %2 sc1" : "=v"(v) : "v"(o), "s"(b) : "memory"); return v; }
DI u32 ld16(const void* b, u32 o) { u32 v; asm volatile("global_load_ushort %0, %1, %2" : "=v"(v) : "v"(o), "s"(b) : "memory"); return v; }
DI u32 ld32(const void* b, u32 o) { u32 v; asm volatile("global_load_dword %0, %1, %2" : "=v"(v) : "v"(o), "s"(b) : "memory"); return v; }
DI u32x2 ld64_sc1(const void* b, u32 o) { u32x2 v; asm volatile("global_load_dwordx2 %0, %1, %2 sc1" : "=v"(v) : "v"(o), "s"(b) : "memory"); return v; }
DI u32x4 ld128_sc1(const void* b, u32 o) { u32x4 v; asm volatile("global_load_dwordx4 %0, %1, %2 sc1" : "=v"(v) : "v"(o), "s"(b) : "memory"); return v; }
DI void st32_sc1(void* b, u32 o, u32 v) { asm volatile("global_store_dword %0, %1, %2 sc1" ::"v"(o), "v"(v), "s"(b) : "memory"); }
DI void st64_sc1(void* b, u32 o, u32x2 v) { asm volatile("global_store_dwordx2 %0, %1, %2 sc1\n\ts_nop 1" ::"v"(o), "v"(v), "s"(b) : "memory"); }
DI void st128_sc1(void* b, u32 o, u32x4 v) { asm volatile("global_store_dwordx4 %0, %1, %2 sc1\n\ts_nop 1" ::"v"(o), "v"(v), "s"(b) : "memory"); }
template <int N>
DI u32 take32(u32 v) { u32 o; asm volatile("s_waitcnt vmcnt(%2)\n\tv_mov_b32 %0, %1" : "=v"(o) : "v"(v), "n"(N) : "memory"); return o; }

DI void poll32(const void* b, u32 o, u32 tag) {
#pragma unroll 1
    for (int it = 0; it < SPIN_CAP; ++it) {
        u32 v;
        asm volatile("global_load_dword %0, %1, %2 sc1\n\ts_waitcnt vmcnt(0)" : "=v"(v) : "v"(o), "s"(b) : "memory");
        if (wall(v == tag)) break;
        __builtin_amdgcn_s_sleep(PSL2);
    }
}
DI void poll32f(const void* b, u32 o, u32 tag) {
#pragma unroll 1
    for (int it = 0; it < SPIN_CAP; ++it) {
        u32 v;
        asm volatile("global_load_dword %0, %1, %2 sc1\n\ts_waitcnt vmcnt(0)" : "=v"(v) : "v"(o), "s"(b) : "memory");
        if (wall(v == tag)) break;
        if (PSL1) __builtin_amdgcn_s_sleep(PSL1);
    }
}
DI void poll128(const void* b, u32 o, u32 tag) {
#pragma unroll 1
    for (int it = 0; it < SPIN_CAP; ++it) {
        u32x4 v;
        asm volatile("global_load_dwordx4 %0, %1, %2 sc1\n\ts_waitcnt vmcnt(0)" : "=v"(v) : "v"(o), "s"(b) : "memory");
        if (wall(v.x == tag && v.y == tag && v.z == tag && v.w == tag)) break;
        __builtin_amdgcn_s_sleep(PSL2);
    }
}
DI bf16x8 trfrag(const char* a0, const char* a1) {
    s16x4 t0 = __builtin_amdgcn_ds_read_tr16_b64_v4i16((LDSP s16x4*)a0);
    s16x4 t1 = __builtin_amdgcn_ds_read_tr16_b64_v4i16((LDSP s16x4*)a1);
    return __builtin_bit_cast(bf16x8, __builtin_shufflevector(t0, t1, 0, 1, 2, 3, 4, 5, 6, 7));
}
DI f32x4 mfma(bf16x8 a, bf16x8 b, f32x4 c) { return __builtin_amdgcn_mfma_f32_16x16x32_bf16(a, b, c, 0, 0, 0); }
DI f32x4 mfmah(f16x8 a, f16x8 b, f32x4 c) { return __builtin_amdgcn_mfma_f32_16x16x32_f16(a, b, c, 0, 0, 0); }
DI f32x16 mfma32(bf16x8 a, bf16x8 b, f32x16 c) { return __builtin_amdgcn_mfma_f32_32x32x16_bf16(a, b, c, 0, 0, 0); }

struct Cplx { float r, i; };
DI Cplx cmul(Cplx a, Cplx b) { return {__builtin_fmaf(a.r, b.r, -(a.i * b.i)), __builtin_fmaf(a.r, b.i, a.i * b.r)}; }
DI Cplx csq(Cplx a) { return {__builtin_fmaf(a.r, a.r, -(a.i * a.i)), (a.r + a.r) * a.i}; }

constexpr int g1_ops(int n) { return ((n >= 1 && n <= 16) ? 1 : 0) + ((n >= 2 && n <= 15) ? 1 : 0); }
constexpr int g1_allow(int n) {
    if (n == 0) return 7;
    if (n == 1) return 6;
    if (n == 2) return 3 + g1_ops(1);
    if (n == 3) return 2 + g1_ops(1) + g1_ops(2);
    if ((n & 1) == 0 && n >= 6) return g1_ops(n - 2) + g1_ops(n - 1);
    return (g1_ops(n - 3) - 1) + g1_ops(n - 2) + g1_ops(n - 1);
}
constexpr int g2_hi(int i) { int m = (10 * (i - 1) + 64) / 8; if (m < 8) m = 8; return m > 34 ? 34 : m; }
constexpr int g2_lo(int i) { return i == 0 ? 8 : g2_hi(i - 1); }
constexpr int g2_ops(int i, int AD) { return (g2_hi(i) - g2_lo(i)) + ((i + AD < 27) ? 1 : 0) + (i == 20 ? 2 : 0) + (i == 23 ? 1 : 0); }
constexpr int g2_allow(int I, int AD) {
    int a = 0;
    if (I < AD) { a = AD - 1 - I; for (int i = 0; i < I; ++i) a += g2_ops(i, AD); }
    else { a = g2_ops(I - AD, AD) - (g2_hi(I - AD) - g2_lo(I - AD)) - 1; for (int i = I - AD + 1; i < I; ++i) a += g2_ops(i, AD); }
    const int need = (10 * I + 9) / 8;
    if (need >= 8) {
        int it = 0;
        while (g2_hi(it) <= need) ++it;
        int b = g2_ops(it, AD) - (need - g2_lo(it)) - 1;
        for (int i = it + 1; i < I; ++i) b += g2_ops(i, AD);
        if (b < a) a = b;
    }
    return a;
}
#define SB() __builtin_amdgcn_sched_barrier(0)

__global__ __launch_bounds__(512, 4) void fused_kernel(
    const u16* __restrict__ x, const u16* __restrict__ Are, const u16* __restrict__ Aim, const u16* __restrict__ Bre,
    const u16* __restrict__ Bim, const u16* __restrict__ Cre, const u16* __restrict__ Cim, const u16* __restrict__ logdt,
    const u16* __restrict__ Dsk, const u16* __restrict__ w1, const u16* __restrict__ b1, const u16* __restrict__ w2,
    u16* __restrict__ out, char* __restrict__ ws, int mask) {
    extern __shared__ __attribute__((aligned(16))) char smem[];
    u64 pc0;
    asm volatile("s_getpc_b64 %0" : "=s"(pc0));
    const int tid = __builtin_amdgcn_workitem_id_x();
    const int l = tid & 63;
    const int w = __builtin_amdgcn_readfirstlane(tid >> 6);
    const int g = l >> 4, c16 = l & 15;
    const int bid = __builtin_amdgcn_workgroup_id_x() ^ SWAPR;
    const u32 lb = (u32)(size_t)(LDSP char*)smem;
    STAMP_DECL;
    STAMP(0);

    auto w2_load = [&](int ks, u32x4 (&wv)[5]) {
#pragma unroll
        for (int i = 0; i < 5; ++i) {
            const int n = i * 64 + l;
            const u32x4* pp = (const u32x4*)((const char*)w2 + (u32)((ks * 32 + n / 10) * 1280 + w * 160 + (n % 10) * 16));
            wv[i] = NT2 ? __builtin_nontemporal_load(pp) : *pp;
        }
    };
    auto w2_store = [&](int ks, const u32x4 (&wv)[5]) {
        char* tile = smem + w * 5632;
#pragma unroll
        for (int i = 0; i < 5; ++i) {
            const int n = i * 64 + l;
            *(u32x4*)(tile + (n / 10) * 176 + (n % 10) * 16) = wv[i];
        }
#pragma unroll
        for (int dd = 0; dd < 5; ++dd) {
            const char* a = tile + (16 * (g >> 1) + 4 * (g & 1) + (c16 >> 2)) * 176 + dd * 32 + 8 * (l & 3);
            const bf16x8 f = trfrag(a, a + 8 * 176);
            st128_sc1(ws, WS_W2T + (u32)(((w * 54 + ks) * 5 + dd) * 1024 + l * 16), __builtin_bit_cast(u32x4, f));
        }
    };

    const bool isG = bid >= 256;
    const int gb = bid & 255;
    const int xcd = gb & 7, idx = gb >> 3;
    const int tk = w >> 2, ch = (w >> 1) & 1, p = w & 1;
    const bool doconv = (mask & 2) && (isG ? (idx >= 28 && tk == 0) : (tk == 0 || idx >= 28));
    const int d0 = 80 * xcd + 2 * (isG ? idx + 8 : idx + 4 * tk);
    const int d = d0 + ch;
    u32 pa[6] = {0, 0, 0, 0, 0, 0}, pld = 0, pdk = 0;
    auto load_params = [&]() {
        const u32 pi = (u32)(d * NM + p * 64 + l);
        pa[0] = Are[pi]; pa[1] = Aim[pi]; pa[2] = Bre[pi]; pa[3] = Bim[pi]; pa[4] = Cre[pi]; pa[5] = Cim[pi];
        pld = logdt[d]; pdk = Dsk[d];
    };
    u32 tag = 0;
    bool pfd = false;
    const int t2 = 8 * (xcd >> 1) + (idx & 7), cg = 4 * (xcd & 1) + (idx >> 3);
    const bool has2 = (mask & 8) != 0;
    const int r32 = l & 31, pw = w & 3;
    const u32 gw2 = WS_W2T + (u32)(cg * 54 * 5120 + l * 16);
    const int i0 = ROT2 ? ((((idx & 7) * 27) >> 3) + (idx >> 3) * 7) % 27 : 0;

    if (isG) {
        __builtin_amdgcn_s_setprio(2);
        const bool has1 = (idx < 28) && (mask & 4);
        const int tt = 4 * (xcd >> 1) + (idx & 3);
        const int ct = 7 * (xcd & 1) + (idx >> 2);
        const int h = l >> 5;
        const bool half = (ct == 13);
        float* csum = (float*)(smem + 73728);

        const int th = pw & 1, fh = pw >> 1, tbe = w >> 2;
        const u32 stoff = WS_STAT + (u32)((tt * 128 + 64 * th + 32 * tbe + r32) * 8);
        auto epilogue1 = [&](const f32x16& a0, const f32x16& a1, u32x2 st0, bool pf) {
#pragma unroll 1
            for (int it = 0; it < SPIN_CAP; ++it) {
                if (wall(st0.y == tag)) break;
                asm volatile("global_load_dwordx2 %0, %1, %2 sc1\n\ts_waitcnt vmcnt(0)" : "=v"(st0) : "v"(stoff), "s"(ws) : "memory");
            }
            STAMP(3);
            const float* cp = csum + fh * 64 + 4 * h;
            const u32 abase = WS_ACT + (u32)(((tt * 8 + 4 * th + 2 * tbe + (r32 >> 4)) * 54 + ct * 4 + fh * 2) * 1024 + (h * 16 + c16) * 16);
            const float nmt = -lo16(st0.x), rt = 1.44269504f * __builtin_amdgcn_rcpf(hi16(st0.x));
            auto epi = [&](const f32x16& a, int c, int g0) {
#pragma unroll
                for (int s = 0; s < 2; ++s) {
                    u32 pk[4];
#pragma unroll
                    for (int mm = 0; mm < 2; ++mm) {
                        const int m = 2 * s + mm;
                        const f32x4 c4 = *(const f32x4*)(cp + c * 32 + 8 * m);
                        const f32x4 b4 = *(const f32x4*)(cp + 128 + c * 32 + 8 * m);
                        float r[4];
#pragma unroll
                        for (int i = 0; i < 4; ++i) {
                            const float h2 = __builtin_fmaf(__builtin_fmaf(nmt, c4[i], a[4 * m + i]), rt, b4[i]);
                            const float e = __builtin_amdgcn_exp2f(h2);
                            const float rr = __builtin_amdgcn_rcpf(__builtin_fmaf(e, e + 2.f, 2.f));
                            r[i] = h2 * __builtin_fmaf(-1.38629436f, rr, 0.69314718f);
                        }
                        pk[2 * mm] = pk2(r[0], r[1]);
                        pk[2 * mm + 1] = pk2(r[2], r[3]);
                    }
                    st128_sc1(ws, abase + c * 1024 + s * 512, u32x4{pk[0], pk[1], pk[2], pk[3]});
                }
            };
            epi(a0, th, 0);
            epi(a1, th ^ 1, 4);
            STAMP(4);
            WAITV(0);
            STAMP(5);
            if (l == 0) st32_sc1(ws, WS_ACTF + (u32)(((tt * 4 + 2 * th + tbe) * 32 + ct * 2 + fh) * 4), tag);
        };
        char* hand = smem + 32768 + pw * 2048 + l * 16;
        const bool eact = !(half && fh == 1);

        if (w >= 4) {
            const int lw = w - 4;
            const u32 ev = ld32_sc1(ws, WS_EPOCH);
            if (has1) {
                u32 gx[2], gw[2];
#pragma unroll
                for (int e = 0; e < 2; ++e) {
                    const int vw = 2 * lw + e;
                    gx[e] = (u32)((tt * 128 + 8 * vw + (l >> 3)) * 1280 + (((l & 7) ^ ((4 * e + g) & 7)) << 4));
                    int wch = c16 ^ ((g << 2) | (vw & 3));
                    if (half) wch &= 7;
                    gw[e] = (u32)((4 * vw + g) * 3456 + ct * 256 + (wch << 4));
                }
                const u32 ldst = lb + lw * 2048;
                const int j0 = ROT1 ? (3 * (idx >> 2) + ((5 * (idx & 3)) >> 1)) % 10 : 0;
                auto issueW = [&](auto NC) {
                    constexpr int N = decltype(NC)::value, j = N >> 1;
                    const int jr = (j + j0 >= 10) ? j + j0 - 10 : j + j0;
                    const u32 o = jr * (64 * 3456) + (N & 1) * (32 * 3456);
                    dma16<0>(w1, gw[0] + o, ldst + (N & 3) * 8192);
                    dma16<0>(w1, gw[1] + o, ldst + (N & 3) * 8192 + 1024);
                };
                auto issueX = [&](auto JC, auto UC) {
                    constexpr int j = decltype(JC)::value, U = decltype(UC)::value;
                    const int jr = (j + j0 >= 10) ? j + j0 - 10 : j + j0;
                    const u32 o = U * (64 * 1280) + jr * 128;
                    dma16<0>(x, gx[0] + o, ldst + 32768 + (j % 3) * 16384 + U * 8192);
                    dma16<0>(x, gx[1] + o, ldst + 32768 + (j % 3) * 16384 + U * 8192 + 1024);
                };
                issueW(IC<0>{}); issueX(IC<0>{}, IC<0>{}); issueX(IC<0>{}, IC<1>{}); issueW(IC<1>{}); issueW(IC<2>{});
                issueX(IC<1>{}, IC<0>{}); issueX(IC<1>{}, IC<1>{}); issueW(IC<3>{}); issueX(IC<2>{}, IC<0>{}); issueX(IC<2>{}, IC<1>{});
                u32 bv = 0, w2flag = 0;
                u32x2 st0;
                auto lstep = [&](auto NC) {
                    constexpr int N = decltype(NC)::value;
                    WAITBAR(2 * g1_allow(N) + (N >= 18 ? 3 : 0));
                    if constexpr (N >= 1 && N <= 16) issueW(IC<N + 3>{});
                    if constexpr (N >= 2 && N <= 15) issueX(IC<N / 2 + 2>{}, IC<N & 1>{});
                    if constexpr (N == 0) {
                        tag = mktag(take32<63>(ev));
                        if (tid == 256) st32_sc1(ws, WS_RD + bid * 4, tag);
                    }
                    if constexpr (N == 17) {
                        bv = ld16(b1, (u32)((ct * 128 + (tid & (half ? 63 : 127))) * 2));
                        st0 = ld64_sc1(ws, stoff);
                        w2flag = ld32_sc1(ws, WS_W2F + (u32)(cg * 256 + (l < 54 ? l : 0) * 4));
                    }
                };
                lstep(IC<0>{}); lstep(IC<1>{}); lstep(IC<2>{}); lstep(IC<3>{}); lstep(IC<4>{}); lstep(IC<5>{}); lstep(IC<6>{});
                lstep(IC<7>{}); lstep(IC<8>{}); lstep(IC<9>{}); lstep(IC<10>{}); lstep(IC<11>{}); lstep(IC<12>{}); lstep(IC<13>{});
                lstep(IC<14>{}); lstep(IC<15>{}); lstep(IC<16>{}); lstep(IC<17>{}); lstep(IC<18>{}); lstep(IC<19>{});
                asm volatile("s_waitcnt vmcnt(0)\n\ts_barrier" : "+v"(bv), "+v"(st0), "+v"(w2flag)::"memory");
                if (tid < 384) csum[128 + (tid & 127)] = bf2f(bv) * 1.44269504f;
                if (tid == 384) csum[256] = asf(tag);
                BAR();
                pfd = has2 && wall(w2flag == tag);
                if (eact) {
                    f32x16 a0, a1;
#pragma unroll
                    for (int i = 0; i < 4; ++i) {
                        const f32x4 u = *(const f32x4*)(hand + (i >> 1) * 8192 + (i & 1) * 1024);
                        const f32x4 v = *(const f32x4*)(hand + (2 + (i >> 1)) * 8192 + (i & 1) * 1024);
#pragma unroll
                        for (int e = 0; e < 4; ++e) { a0[4 * i + e] = u[e]; a1[4 * i + e] = v[e]; }
                    }
                    WAITL0();
                    epilogue1(a0, a1, st0, false);
                }
            } else {
                tag = mktag(take32<0>(ev));
            }
        } else if (has1) {
            const int xa = 32768 + th * 8192 + r32 * 128 + ((h ^ ((r32 >> 1) & 7)) << 4);
            int wa[2];
            {
                const int q = c16 >> 2, p = l & 3, blk = (l >> 4) & 1;
#pragma unroll
                for (int t = 0; t < 2; ++t) {
                    const int row = 8 * h + 4 * t + q;
                    const int f = (q << 2) | ((2 * h + t) & 3);
                    wa[t] = (row * 256 + (((2 * blk + (p >> 1)) ^ f) << 4) + 8 * (p & 1)) ^ (fh << 7) ^ (th << 6);
                }
            }
            f32x16 acc[2][2];
#pragma unroll
            for (int i = 0; i < 16; ++i) { acc[0][0][i] = 0.f; acc[0][1][i] = 0.f; acc[1][0][i] = 0.f; acc[1][1][i] = 0.f; }
            float cs = 0.f, cs2 = 0.f;
            u32x2 st0;
            const bf16x2 ones = __builtin_bit_cast(bf16x2, 0x3F803F80u);
            bf16x8 xf[2][2], wf[2][2];
            auto cstep = [&](auto MC) {
                constexpr int M = decltype(MC)::value, N = M >> 1, B = M & 1, P = B ^ 1;
                auto rdx = [&](int tb) { if constexpr (M < 40) xf[B][tb] = *(const bf16x8*)(smem + ((M >> 2) % 3) * 16384 + tb * 4096 + (xa ^ ((M & 3) << 5))); };
                auto rdw = [&](int c) {
                    if constexpr (M < 40) wf[B][c] = trfrag(smem + (N & 3) * 8192 + B * 4096 + (wa[0] ^ (c << 6)), smem + (N & 3) * 8192 + B * 4096 + (wa[1] ^ (c << 6)));
                };
                SB();
                if constexpr (B == 0) BAR();
                if constexpr (M == 0) STAMP(1);
                if constexpr (M >= 1) acc[0][0] = mfma32(wf[P][0], xf[P][0], acc[0][0]);
                SB();
                rdx(0); rdw(0);
                SB();
                if constexpr (M >= 1) acc[1][0] = mfma32(wf[P][0], xf[P][1], acc[1][0]);
                SB();
                rdx(1); rdw(1);
                SB();
                if constexpr (M >= 1) acc[0][1] = mfma32(wf[P][1], xf[P][0], acc[0][1]);
                SB();
                if constexpr (M >= 1) {
                    const u32x4 wv = __builtin_bit_cast(u32x4, wf[P][0]);
                    const u32 a0 = wv[0], a1 = wv[1], a2_ = wv[2], a3 = wv[3];
                    cs = __builtin_amdgcn_fdot2_f32_bf16(__builtin_bit_cast(bf16x2, a0), ones, cs, false);
                    cs2 = __builtin_amdgcn_fdot2_f32_bf16(__builtin_bit_cast(bf16x2, a1), ones, cs2, false);
                    cs = __builtin_amdgcn_fdot2_f32_bf16(__builtin_bit_cast(bf16x2, a2_), ones, cs, false);
                    cs2 = __builtin_amdgcn_fdot2_f32_bf16(__builtin_bit_cast(bf16x2, a3), ones, cs2, false);
                }
                if constexpr (M == 36) st0 = ld64_sc1(ws, stoff);
                SB();
                if constexpr (M >= 1) acc[1][1] = mfma32(wf[P][1], xf[P][1], acc[1][1]);
            };
#define C4(m) cstep(IC<m>{}); cstep(IC<m + 1>{}); cstep(IC<m + 2>{}); cstep(IC<m + 3>{});
            C4(0) C4(4) C4(8) C4(12) C4(16) C4(20) C4(24) C4(28) C4(32) C4(36)
#undef C4
            cstep(IC<40>{});
            STAMP(2);
            cs += cs2;
            cs += asf((u32)__builtin_amdgcn_ds_bpermute((l ^ 32) * 4, (int)asu(cs)));
            if (l < 32) csum[fh * 64 + th * 32 + l] = cs;
#pragma unroll
            for (int i = 0; i < 4; ++i) {
                *(f32x4*)(hand + (i >> 1) * 8192 + (i & 1) * 1024) = f32x4{acc[1][0][4 * i], acc[1][0][4 * i + 1], acc[1][0][4 * i + 2], acc[1][0][4 * i + 3]};
                *(f32x4*)(hand + (2 + (i >> 1)) * 8192 + (i & 1) * 1024) = f32x4{acc[1][1][4 * i], acc[1][1][4 * i + 1], acc[1][1][4 * i + 2], acc[1][1][4 * i + 3]};
            }
            asm volatile("s_waitcnt vmcnt(0) lgkmcnt(0)\n\ts_barrier" : "+v"(st0)::"memory");
            tag = asu(csum[256]);
            if (eact) epilogue1(acc[0][0], acc[0][1], st0, false);
        }
        if (idx >= 28) {
            if (w < 4) { const u32 ev = ld32_sc1(ws, WS_EPOCH); tag = mktag(take32<0>(ev)); }
            if (doconv) load_params();
            if (mask & 1) {
                const int k0 = gb - 224;
                u32x4 wv[5], wv2[5];
                w2_load(k0, wv);
                if (k0 < 22) w2_load(k0 + 32, wv2);
                w2_store(k0, wv);
                if (k0 < 22) w2_store(k0 + 32, wv2);
                WAITV(0);
                if (l == 0) st32_sc1(ws, WS_W2F + (u32)((w * 64 + k0) * 4), tag);
                if (k0 < 22 && l == 0) st32_sc1(ws, WS_W2F + (u32)((w * 64 + k0 + 32) * 4), tag);
            }
            BAR();
            if (tid == 0) st32_sc1(ws, WS_RD + bid * 4, tag);
        } else if (!has1 && w < 4) {
            const u32 ev = ld32_sc1(ws, WS_EPOCH);
            tag = mktag(take32<0>(ev));
        }

    } else {
    const int cb = bid;
    const int tg = 64 * (xcd >> 1) + 32 * (xcd & 1) + idx;
    u32 xv[5];
    if (mask & 1) {
        const u32* xr = (const u32*)x + (u32)((tg * 8 + w) * 320 + l);
#pragma unroll
        for (int i = 0; i < 5; ++i) xv[i] = xr[64 * i];
    }
    u32 cw[CWARM];
    if (w == 6 && idx == 1) {
#pragma unroll
        for (int i = 0; i < CWARM; ++i)
            asm volatile("global_load_dword %0, %1, %2" : "=v"(cw[i]) : "v"((u32)((i * 64 + l) * 128)), "s"(pc0) : "memory");
    }
    const u32 ev = ld32_sc1(ws, WS_EPOCH);
    if (doconv) load_params();
    char* lnt = smem + 65536;
    float mb = 0.f, den = 1.f;
    if (mask & 1) {
        float s = 0.f;
#pragma unroll
        for (int i = 0; i < 5; ++i) s += lo16(xv[i]) + hi16(xv[i]);
        const float mean = wave_sum(s) * (1.f / 640.f);
        float q = 0.f;
#pragma unroll
        for (int i = 0; i < 5; ++i) {
            const float a = lo16(xv[i]) - mean, b = hi16(xv[i]) - mean;
            q = __builtin_fmaf(a, a, q); q = __builtin_fmaf(b, b, q);
        }
        const float var = wave_sum(q) * (1.f / 640.f);
        mb = rbf(mean);
        den = rbf(__builtin_sqrtf(rbf(rbf(var) + 1e-5f)));
        const float rden = 1.0f / den;
        char* lw = lnt + w * 1280 + l * 4 + w * 64;
#pragma unroll
        for (int i = 0; i < 5; ++i) {
            const u32 dd = pk2(lo16(xv[i]) - mb, hi16(xv[i]) - mb);
            const u32 o = pk2(lo16(dd) * rden, hi16(dd) * rden);
            if (i < 3) *(u32*)(lw + 256 * i) = o;
            else *(u32*)(lw + 256 * i - ((256 * i + l * 4 + w * 64 >= 1280) ? 1280 : 0)) = o;
        }
    }
    tag = mktag(doconv ? take32<8>(ev) : take32<0>(ev));
#pragma unroll
    for (int i = 0; i < CWARM; ++i) asm volatile("" ::"v"(cw[i]));
    STAMP(1);
    if ((mask & 1) && l == 0) st64_sc1(ws, WS_STAT + (u32)((tg * 8 + w) * 8), u32x2{pk2(mb, den), tag});
    BAR();
    if (tid == 0) st32_sc1(ws, WS_RD + bid * 4, tag);
    if (mask & 1) {
        const int q = c16 >> 2;
        auto piece = [&](int i) {
            int p0 = 2 * (i * 4 + g) + 4 * q, p1 = p0 + 16;
            if (p0 >= 80) p0 -= 80;
            if (p1 >= 80) p1 -= 80;
            const bf16x8 f = trfrag(lnt + q * 1280 + p0 * 16 + 8 * (l & 3), lnt + (q + 4) * 1280 + p1 * 16 + 8 * (l & 3));
            st128_sc1(ws, WS_XB + (u32)(tg * 10240 + i * 1024 + l * 16), __builtin_bit_cast(u32x4, f));
        };
        piece(w);
        if (w >= 6) piece(2 + w);
    }
    STAMP(2);
    if (!doconv) {
        WAITBAR(0);
        if (!(mask & 2) && (mask & 1) && tid == 0) st32_sc1(ws, WS_XBF + cb * 4, tag);
        if (cb == 0 && w == 7) {
            poll128(ws, WS_RD + l * 16, tag);
            poll128(ws, WS_RD + 1024 + l * 16, tag);
            if (l == 0) st32_sc1(ws, WS_EPOCH, tag);
        }
        return;
    }
    }
    if (doconv) {
        const float dt = rbf(__builtin_expf(bf2f(pld)));
        const float lr2 = (dt * bf2f(pa[0])) * 1.44269504f;
        const float li = dt * bf2f(pa[1]);
        const float rh = li * 0.15915494f;
        const float rl = __builtin_fmaf(li, 0.15915494f, -rh) + li * 3.0908620e-9f;
        auto E = [&](float t) {
            const float ph = rh * t;
            const float er = __builtin_fmaf(rl, t, __builtin_fmaf(rh, t, -ph));
            const float fr = (ph - __builtin_rintf(ph)) + er;
            const float m = __builtin_amdgcn_exp2f(lr2 * t);
            return Cplx{m * __builtin_amdgcn_cosf(fr), m * __builtin_amdgcn_sinf(fr)};
        };
        char* scr = smem + w * 4608;
        auto put = [&](int row, Cplx v) { *(u32*)(scr + row * 288 + l * 4) = pkh(v.r, v.i); };
        const char* frd = scr + c16 * 288 + g * 16;
        f16x8 bq[4];
        float inv_scale;
        {
            Cplx c = cmul(Cplx{bf2f(pa[4]), bf2f(pa[5])}, Cplx{dt * bf2f(pa[2]), dt * bf2f(pa[3])});
            const float mx = wave_max(__builtin_fmaxf(__builtin_fabsf(c.r), __builtin_fabsf(c.i)));
            int ex = 14 - __builtin_amdgcn_frexp_expf(mx);
            ex = ex > 100 ? 100 : (ex < -100 ? -100 : ex);
            inv_scale = __builtin_amdgcn_ldexpf(1.f, -ex);
            c.r = __builtin_amdgcn_ldexpf(c.r, ex);
            c.i = -__builtin_amdgcn_ldexpf(c.i, ex);
            Cplx e8 = E(8.f);
            e8.i = -e8.i;
            const Cplx e16 = csq(e8), e32 = csq(e16), e64 = csq(e32);
            Cplx b[8];
            b[0] = c; b[1] = cmul(c, e8); b[2] = cmul(c, e16); b[3] = cmul(b[1], e16);
#pragma unroll
            for (int j = 0; j < 4; ++j) b[4 + j] = cmul(b[j], e32);
#pragma unroll
            for (int j = 0; j < 8; ++j) { put(j, b[j]); put(8 + j, cmul(b[j], e64)); }
#pragma unroll
            for (int s = 0; s < 4; ++s) bq[s] = *(const f16x8*)(frd + s * 64);
        }
        WAITBAR(0);
        if (tid == 0 && (mask & 1) && !isG) st32_sc1(ws, WS_XBF + gb * 4, tag);
        u32 ix[9];
#pragma unroll
        for (int k = 0; k < 9; ++k) ix[k] = g_idx.v[k * 128 + p * 64 + l];
        auto tile_mm = [&]() {
            f32x4 c = {0, 0, 0, 0};
#pragma unroll
            for (int s = 0; s < 4; ++s) c = mfmah(*(const f16x8*)(frd + s * 64), bq[s], c);
            return c;
        };
        f32x4 T0, T1, T2;
        {
            const Cplx e1 = E(1.f), e2 = csq(e1), e3 = cmul(e2, e1), e4 = csq(e2), e5 = cmul(e4, e1), e6 = cmul(e4, e2), e7 = cmul(e4, e3);
            const Cplx e128 = E(128.f);
            put(0, Cplx{1.f, 0.f}); put(1, e1); put(2, e2); put(3, e3); put(4, e4); put(5, e5); put(6, e6); put(7, e7);
            put(8, e128); put(9, cmul(e128, e1)); put(10, cmul(e128, e2)); put(11, cmul(e128, e3));
            put(12, cmul(e128, e4)); put(13, cmul(e128, e5)); put(14, cmul(e128, e6)); put(15, cmul(e128, e7));
            T0 = tile_mm();
            const Cplx e256 = csq(e128), e384 = cmul(e256, e128), e512 = csq(e256);
            const Cplx e640 = cmul(e512, e128), e768 = cmul(e512, e256), e896 = cmul(e512, e384);
            put(0, e256); put(1, cmul(e256, e2)); put(2, cmul(e256, e4)); put(3, cmul(e256, e6));
            put(4, e384); put(5, cmul(e384, e2)); put(6, cmul(e384, e4)); put(7, cmul(e384, e6));
            put(8, e512); put(9, cmul(e512, e4)); put(10, e640); put(11, cmul(e640, e4));
            put(12, e768); put(13, cmul(e768, e4)); put(14, e896); put(15, cmul(e896, e4));
            T1 = tile_mm();
            const Cplx e1k = E(1024.f);
            put(0, e1k); put(1, cmul(e1k, e128)); put(2, cmul(e1k, e256)); put(3, cmul(e1k, e384)); put(4, cmul(e1k, e512));
            put(5, cmul(e1k, e640)); put(6, cmul(e1k, e768)); put(7, cmul(e1k, e896)); put(8, csq(e1k));
            T2 = tile_mm();
        }
        STAMP(3);
        {
            float* tabp = (float*)scr;
            const int j = c16;
            *(f32x4*)(tabp + 128 * (g >> 1) + 4 * (g & 1) + 8 * j) = T0 * inv_scale;
            T1 *= inv_scale;
            T2 *= inv_scale;
            if (g < 2) {
                *(f32x4*)(tabp + 256 + 64 * g + 4 * j) = T1;
#pragma unroll
                for (int i = 0; i < 4; ++i) tabp[512 + 16 * (4 * g + i) + j] = T2[i];
            } else {
                *(f32x2*)(tabp + 384 + 64 * (g - 2) + 2 * j) = f32x2{T1[0], T1[1]};
                *(f32x2*)(tabp + 384 + 64 * (g - 2) + 32 + 2 * j) = f32x2{T1[2], T1[3]};
                if (g == 2) tabp[640 + j] = T2[0];
            }
        }
        if (w == 0) poll128(ws, WS_XBF + l * 16, tag);
        BAR();
        STAMP(4);
        char* U = smem + 36864 + tk * 9216;
#pragma unroll
        for (int i = 0; i < 2; ++i) {
            const int c = ((w & 3) * 2 + i) * 32 + (l >> 1);
            const int chs = (l & 1) ^ ((c >> 3) & 1);
            dma16<1>(ws, WS_XB + (u32)(c * 10240 + (d0 + chs) * 16), lb + 36864 + tk * 9216 + 1024 + ((w & 3) * 2 + i) * 1024);
        }
        if ((w & 3) == 0) *(u32x4*)(U + l * 16) = u32x4{0, 0, 0, 0};
        char* tab = smem + 76288 + (w >> 1) * 1344;
        {
            const float* t0 = (const float*)(smem + (w & 6) * 4608);
            const float* t1 = (const float*)(smem + (w | 1) * 4608);
#pragma unroll
            for (int it = 0; it < 3; ++it) {
                const int i = it * 128 + p * 64 + l;
                if (i < 328) {
                    const f32x2 a = *(const f32x2*)(t0 + 2 * i), b = *(const f32x2*)(t1 + 2 * i);
                    *(u32*)(tab + 4 * i) = pk2(a.x + b.x, a.y + b.y);
                } else if (i == 330) *(u32*)(tab + 4 * i) = 0u;
            }
        }
        BAR();
        STAMP(5);
        char* KE = smem + (w >> 1) * 8448;
        char* KO = KE + 4160;
        {
            const int m = p * 64 + l;
            u32 e[9];
#pragma unroll
            for (int k = 0; k < 9; ++k) {
                u16x2 v;
                v.x = *(const u16*)(tab + (ix[k] & 0xFFFFu));
                v.y = *(const u16*)(tab + (ix[k] >> 16));
                e[k] = __builtin_bit_cast(u32, v);
            }
            *(u32x4*)(KE + m * 32) = u32x4{e[0], e[1], e[2], e[3]};
            *(u32x4*)(KE + m * 32 + 16) = u32x4{e[4], e[5], e[6], e[7]};
            u32 o[8];
#pragma unroll
            for (int k = 0; k < 8; ++k) o[k] = __builtin_amdgcn_alignbit(e[k + 1], e[k], 16);
            *(u32x4*)(KO + m * 32) = u32x4{o[0], o[1], o[2], o[3]};
            *(u32x4*)(KO + m * 32 + 16) = u32x4{o[4], o[5], o[6], o[7]};
            if (p == 0 && l < 4) *(u32x4*)(KE + 4096 + (l & 1) * 16 + (l >> 1) * 4160) = u32x4{0, 0, 0, 0};
        }
        WAITBAR(0);
        STAMP(6);
        f32x4 y[8];
#pragma unroll
        for (int a = 0; a < 8; ++a) y[a] = f32x4{0, 0, 0, 0};
#pragma unroll 1
        for (int rr = 0; rr < 4; ++rr) {
            const int r = 8 * p + 2 * rr;
            const int c = 2 * (c16 - r - 1) + g + 32;
            const char* ub = U + c * 32 + ((ch ^ ((c >> 3) & 1)) << 4);
            bf16x8 B[8];
#pragma unroll
            for (int a = 0; a < 8; ++a) B[a] = *(const bf16x8*)(ub + a * 1024);
            const int i0 = 2031 - 16 * r - c16 + 8 * g;
            const char* kb = ((i0 & 1) ? KO : KE) + (i0 >> 1) * 4;
#pragma unroll
            for (int q = 0; q < 8; ++q) {
                const u32x4a av = *(const u32x4a*)(kb - q * 512);
                const bf16x8 A = __builtin_bit_cast(bf16x8, av);
#pragma unroll
                for (int a = q; a < 8; ++a) y[a] = mfma(A, B[a - q], y[a]);
            }
        }
        STAMP(7);
        const float dsk = bf2f(pdk);
        BAR();
        char* ew = smem + w * 4096 + l * 16;
        const char* eo = smem + (w ^ 1) * 4096 + l * 16;
        char* so = smem + 55296 + w * 2048;
        auto fin = [&](int a, f32x4 yy) {
            yy += *(const f32x4*)(eo + a * 1024);
            const int cu = 2 * (16 * (4 * p + a) + c16) + (g >> 1) + 32;
            const u32x2 uv = *(const u32x2*)(U + cu * 32 + ((ch ^ ((cu >> 3) & 1)) << 4) + (g & 1) * 8);
            *(u32x2*)(so + a * 512 + c16 * 32 + g * 8) =
                u32x2{pk2(__builtin_fmaf(lo16(uv.x), dsk, yy[0]), __builtin_fmaf(hi16(uv.x), dsk, yy[1])),
                      pk2(__builtin_fmaf(lo16(uv.y), dsk, yy[2]), __builtin_fmaf(hi16(uv.y), dsk, yy[3]))};
        };
        if (p == 0) {
#pragma unroll
            for (int a = 0; a < 4; ++a) *(f32x4*)(ew + a * 1024) = y[4 + a];
            BAR();
#pragma unroll
            for (int a = 0; a < 4; ++a) fin(a, y[a]);
        } else {
#pragma unroll
            for (int a = 0; a < 4; ++a) *(f32x4*)(ew + a * 1024) = y[a];
            BAR();
#pragma unroll
            for (int a = 0; a < 4; ++a) fin(a, y[4 + a]);
        }
#pragma unroll
        for (int i = 0; i < 2; ++i) {
            const u32x4 v = *(const u32x4*)(so + i * 1024 + l * 16);
            st128_sc1(ws, WS_ST + (u32)(d * 4096 + p * 2048 + i * 1024 + l * 16), v);
        }
        STAMP(8);
        WAITV(0);
        if (l == 0) st32_sc1(ws, WS_SF + (u32)((p * 640 + d) * 4), tag);
        STAMP(9);
        if (!isG) STAMP_FLUSH();
    } else if (isG && idx >= 28 && (mask & 2)) {
        BAR(); BAR(); BAR(); BAR(); BAR(); BAR();
    }
    if (!isG) return;
    {
        auto rot = [&](int I) { return (I + i0 >= 27) ? I + i0 - 27 : I + i0; };
        auto pf_w2 = [&](int m) {
#pragma unroll
            for (int e = 0; e < 2; ++e) {
                int f = 10 * i0 + 8 * m + 2 * pw + e;
                if (f >= 270) f -= 270;
                dma16<1>(ws, gw2 + f * 1024, lb + m * 8192 + (2 * pw + e) * 1024);
            }
        };
        if (has2) {
            const int tbk = w & 3, kp = w >> 2;
            BAR();
            if (w >= 4) {
                if (!pfd) poll32(ws, WS_W2F + (u32)(cg * 256 + (l < 54 ? l : 0) * 4), tag);
#pragma unroll
                for (int m = 0; m < 8; ++m) pf_w2(m);
            }
            int fsrc = 10 * i0 + w + 64;
            if (fsrc >= 270) fsrc -= 270;
            auto issue_w2 = [&](auto MC) {
                constexpr int M = decltype(MC)::value;
                if (M < 33 || w < 6) dma16<1>(ws, gw2 + fsrc * 1024, lb + (M & 7) * 8192 + w * 1024);
                else dma16<1>(ws, gw2, lb + 73728 + 2048 + (w & 1) * 1024);
                fsrc += 8;
                if (fsrc >= 270) fsrc -= 270;
            };
            STAMP(6);
            u32 rmask = 0;
            const u32 afo = WS_ACTF + (u32)(((t2 * 2 + (tbk >> 1)) * 32 + r32) * 4);
            auto need = [&](int bit) {
                if (__builtin_expect(!((rmask >> bit) & 1u), 0)) {
#pragma unroll 1
                    for (int it = 0; it < SPIN_CAP; ++it) {
                        u32 v;
                        asm volatile("global_load_dword %0, %1, %2 sc1\n\ts_waitcnt vmcnt(0)" : "=v"(v) : "v"(afo), "s"(ws) : "memory");
                        rmask = (u32)__builtin_amdgcn_ballot_w64(v == tag);
                        if ((rmask >> bit) & 1u) break;
                        __builtin_amdgcn_s_sleep(PSL1);
                    }
                }
            };
            const u32 ga = WS_ACT + (u32)(((4 * t2 + tbk) * 54 + kp) * 1024 + l * 16);
            constexpr int AD = 6;
            u32x4 af[8];
#pragma unroll
            for (int i = 0; i < AD; ++i) { need(rot(i)); af[i] = ld128_sc1(ws, ga + rot(i) * 2048); }
            f32x4 a2[5];
#pragma unroll
            for (int d = 0; d < 5; ++d) a2[d] = f32x4{0, 0, 0, 0};
            const int wl = kp * 5120 + l * 16;
            u32 sf0 = 0, sf1 = 0;
            bool sdone = false;
            const int n0 = w * 64 + l, n1 = 512 + (w & 1) * 64 + l;
            const u32 gx0 = (u32)((t2 * 64 + n0 / 10) * 1280 + cg * 160 + (n0 % 10) * 16);
            const u32 gx1 = (u32)((t2 * 64 + n1 / 10) * 1280 + cg * 160 + (n1 % 10) * 16);
            const u32 gs0 = WS_ST + (u32)((cg * 80 + (n0 >> 3)) * 4096 + t2 * 128 + (n0 & 7) * 16);
            const u32 gs1 = WS_ST + (u32)((cg * 80 + (n1 >> 3)) * 4096 + t2 * 128 + (n1 & 7) * 16);
            constexpr int XS = 16384, SS = 65536;
            const u32 sfp = WS_SF + (u32)(((t2 >> 4) * 640 + cg * 80) * 4);
            bf16x8 wq[2][5];
            auto load_s = [&]() {
                dma16<1>(ws, gs0, lb + SS + w * 1024);
                if (w < 2) dma16<1>(ws, gs1, lb + SS + 8192 + w * 1024);
            };
            auto st2 = [&](auto IC_) {
                constexpr int I = decltype(IC_)::value, B = I & 1;
                constexpr int allow = g2_allow(I, AD);
                SB();
                if constexpr (I == 24)
                    asm volatile("s_waitcnt vmcnt(%3)\n\ts_barrier" : "+v"(af[I & 7]), "+v"(sf0), "+v"(sf1) : "n"(allow < g2_ops(21, AD) + g2_ops(22, AD) + g2_ops(23, AD) ? allow : g2_ops(21, AD) + g2_ops(22, AD) + g2_ops(23, AD)) : "memory");
                else
                    asm volatile("s_waitcnt vmcnt(%1)\n\ts_barrier" : "+v"(af[I & 7]) : "n"(allow) : "memory");
                const bf16x8 bfrag = __builtin_bit_cast(bf16x8, af[(I + 7) & 7]);
#pragma unroll
                for (int d = 0; d < 5; ++d) {
                    if constexpr (I >= 1) a2[d] = mfma(wq[B ^ 1][d], bfrag, a2[d]);
                    SB();
                    {
                        const int o0 = ((10 * I + d) & 63) * 1024;
                        if (o0 + 5120 + 1024 <= 65536) wq[B][d] = *(const bf16x8*)(smem + wl + o0);
                        else wq[B][d] = *(const bf16x8*)(smem + ((wl + o0) & 65535));
                    }
                    if (d == 0) { if constexpr (g2_lo(I) < g2_hi(I)) issue_w2(IC<g2_lo(I)>{}); }
                    if (d == 1) { if constexpr (g2_lo(I) + 1 < g2_hi(I)) issue_w2(IC<g2_lo(I) + 1>{}); }
                    if (d == 2) { if constexpr (I + AD < 27) { need(rot(I + AD)); af[(I + AD) & 7] = ld128_sc1(ws, ga + rot(I + AD) * 2048); } }
                    SB();
                }
                if constexpr (I == 20) {
                    sf0 = ld32_sc1(ws, sfp + l * 4);
                    sf1 = ld32_sc1(ws, sfp + 256 + c16 * 4);
                }
                if constexpr (I == 23) {
                    dma16<0>(x, gx0, lb + XS + w * 1024);
                    if (w < 2) dma16<0>(x, gx1, lb + XS + 8192 + w * 1024);
                }
                if constexpr (I == 24) {
                    sdone = wall(sf0 == tag && sf1 == tag);
                    if (sdone) load_s();
                }
            };
            st2(IC<0>{}); st2(IC<1>{}); st2(IC<2>{}); st2(IC<3>{}); st2(IC<4>{}); st2(IC<5>{}); st2(IC<6>{}); st2(IC<7>{}); st2(IC<8>{});
            st2(IC<9>{}); st2(IC<10>{}); st2(IC<11>{}); st2(IC<12>{}); st2(IC<13>{}); st2(IC<14>{}); st2(IC<15>{}); st2(IC<16>{});
            st2(IC<17>{}); st2(IC<18>{}); st2(IC<19>{}); st2(IC<20>{}); st2(IC<21>{}); st2(IC<22>{}); st2(IC<23>{}); st2(IC<24>{});
            st2(IC<25>{}); st2(IC<26>{});
            {
                const bf16x8 bfrag = __builtin_bit_cast(bf16x8, af[26 & 7]);
#pragma unroll
                for (int d = 0; d < 5; ++d) a2[d] = mfma(wq[0][d], bfrag, a2[d]);
            }
            STAMP(7);
            if (!sdone) {
                poll32(ws, sfp + l * 4, tag);
                poll32(ws, sfp + 256 + c16 * 4, tag);
                load_s();
            }
            char* ex = smem + 30720 + tbk * 5120 + l * 16;
            if (kp == 0) { *(f32x4*)(ex + 3072) = a2[3]; *(f32x4*)(ex + 4096) = a2[4]; }
            else { *(f32x4*)(ex) = a2[0]; *(f32x4*)(ex + 1024) = a2[1]; *(f32x4*)(ex + 2048) = a2[2]; }
            WAITBAR(0);
            STAMP(8);
            auto fin = [&](int d, f32x4 o) {
                o += *(const f32x4*)(ex + d * 1024);
                char* xp = smem + XS + (16 * tbk + c16) * 160 + d * 32 + 8 * g;
                const u32x2 xv = *(const u32x2*)xp;
                const s16x4 sv4 = __builtin_amdgcn_ds_read_tr16_b64_v4i16(
                    (LDSP s16x4*)(smem + SS + (16 * d + 4 * g + (c16 >> 2)) * 128 + tbk * 32 + 8 * (l & 3)));
                const u32x2 sv = __builtin_bit_cast(u32x2, sv4);
                const float xs[4] = {lo16(xv.x), hi16(xv.x), lo16(xv.y), hi16(xv.y)};
                const float ss[4] = {lo16(sv.x), hi16(sv.x), lo16(sv.y), hi16(sv.y)};
                float r[4];
#pragma unroll
                for (int i = 0; i < 4; ++i) {
                    const float sg = __builtin_amdgcn_rcpf(1.f + __builtin_amdgcn_exp2f(-1.44269504f * o[i]));
                    r[i] = __builtin_fmaf(ss[i] * o[i], sg, xs[i]);
                }
                *(u32x2*)xp = u32x2{pk2(r[0], r[1]), pk2(r[2], r[3])};
            };
            if (kp == 0) { fin(0, a2[0]); fin(1, a2[1]); fin(2, a2[2]); }
            else { fin(3, a2[3]); fin(4, a2[4]); }
            BAR();
            *(u32x4*)((char*)out + gx0) = *(const u32x4*)(smem + XS + n0 * 16);
            if (w < 2) *(u32x4*)((char*)out + gx1) = *(const u32x4*)(smem + XS + n1 * 16);
        }
        STAMP(9);
        if (mask & 12) STAMP_FLUSH();
    }
}
#if defined(__HIP_DEVICE_COMPILE__)
#pragma clang attribute pop
#endif

extern "C" void kernel_launch(void* const* d_in, const int* in_sizes, int n_in, void* d_out, int out_size, void* d_ws,
                              size_t ws_size, hipStream_t stream) {
    (void)hipFuncSetAttribute((const void*)fused_kernel, hipFuncAttributeMaxDynamicSharedMemorySize, LDS_BYTES);
#define A(i) (const u16*)d_in[i]
    fused_kernel<<<512, 512, LDS_BYTES, stream>>>(A(0), A(1), A(2), A(3), A(4), A(5), A(6), A(7), A(8), A(10), A(11), A(12),
                                                  (u16*)d_out, (char*)d_ws, 15);
}
```

```cpp
#include <hip/hip_runtime.h>
#include <stdint.h>
#include <type_traits>

typedef unsigned short u16;
typedef unsigned int u32;
typedef unsigned long long u64;
typedef __attribute__((ext_vector_type(8))) __bf16 bf16x8;
typedef __attribute__((ext_vector_type(2))) __bf16 bf16x2;
typedef __attribute__((ext_vector_type(8))) _Float16 f16x8;
typedef __attribute__((ext_vector_type(2))) _Float16 f16x2;
typedef __attribute__((ext_vector_type(4))) short s16x4;
typedef __attribute__((ext_vector_type(2))) u16 u16x2;
typedef __attribute__((ext_vector_type(4))) float f32x4;
typedef __attribute__((ext_vector_type(2))) float f32x2;
typedef __attribute__((ext_vector_type(4))) u32 u32x4;
typedef __attribute__((ext_vector_type(2))) u32 u32x2;
typedef __attribute__((ext_vector_type(4), aligned(4))) u32 u32x4a;
#define LDSP __attribute__((address_space(3)))
#define DI __device__ __forceinline__
template <int N> using IC = std::integral_constant<int, N>;

constexpr int NM = 128;
constexpr u32 WS_EPOCH = 0, WS_RD = 1024, WS_XBF = 4096, WS_W2F = 5120, WS_SF = 8192, WS_ACTF = 16384, WS_STAT = 32768;
constexpr u32 WS_XB = 1u << 20, WS_ST = 4u << 20, WS_W2T = 8u << 20, WS_ACT = 12u << 20;
constexpr int LDS_BYTES = 81920;
constexpr int SPIN_CAP = 1 << 16;

struct IdxTab {
    u32 v[9 * 128];
    static constexpr int idx(int l) {
        if (l < 0) return 660;
        if (l < 256) return l;
        int sh = l < 512 ? 1 : (l < 1024 ? 2 : 3);
        int q = (l + ((1 << (sh - 1)) - 1) + ((l >> sh) & 1)) >> sh;
        return 128 * sh + q;
    }
    constexpr IdxTab() : v() {
        for (int k = 0; k < 9; ++k)
            for (int m = 0; m < 128; ++m)
                v[k * 128 + m] = (u32)(2 * idx(2047 - (16 * m + 2 * k))) | ((u32)(2 * idx(2047 - (16 * m + 2 * k + 1))) << 16);
    }
};
__device__ const IdxTab g_idx = IdxTab();

#if defined(__HIP_DEVICE_COMPILE__)
#pragma clang attribute push(__attribute__((target("no-packed-fp32-ops"))), apply_to = function)
#endif
DI float asf(u32 v) { return __builtin_bit_cast(float, v); }
DI u32 asu(float v) { return __builtin_bit_cast(u32, v); }
DI bool wall(bool p) { return __builtin_amdgcn_ballot_w64(p) == ~0ull; }
DI float bf2f(u32 v) { return asf(v << 16); }
DI u32 pk2(float a, float b) { f32x2 f = {a, b}; return __builtin_bit_cast(u32, __builtin_convertvector(f, bf16x2)); }
DI u32 pkh(float a, float b) { f32x2 f = {a, b}; return __builtin_bit_cast(u32, __builtin_convertvector(f, f16x2)); }
DI float rbf(float a) { return asf(pk2(a, a) << 16); }
DI float lo16(u32 v) { return asf(v << 16); }
DI float hi16(u32 v) { return asf(v & 0xFFFF0000u); }
DI u32 mktag(u32 e) { return e * 0x9E3779B1u + 0x7F4A7C15u; }
template <int CTRL, int RM>
DI float dpp(float v) { return asf((u32)__builtin_amdgcn_update_dpp(0, (int)asu(v), CTRL, RM, 0xf, true)); }
DI float wave_sum(float v) {
    v += dpp<0x111, 0xf>(v); v += dpp<0x112, 0xf>(v); v += dpp<0x114, 0xf>(v); v += dpp<0x118, 0xf>(v);
    v += dpp<0x142, 0xa>(v); v += dpp<0x143, 0xc>(v);
    return asf((u32)__builtin_amdgcn_readlane((int)asu(v), 63));
}
DI float wave_max(float v) {
    v = __builtin_fmaxf(v, dpp<0x111, 0xf>(v)); v = __builtin_fmaxf(v, dpp<0x112, 0xf>(v));
    v = __builtin_fmaxf(v, dpp<0x114, 0xf>(v)); v = __builtin_fmaxf(v, dpp<0x118, 0xf>(v));
    v = __builtin_fmaxf(v, dpp<0x142, 0xa>(v)); v = __builtin_fmaxf(v, dpp<0x143, 0xc>(v));
    return asf((u32)__builtin_amdgcn_readlane((int)asu(v), 63));
}

#define WAITV(n) asm volatile("s_waitcnt vmcnt(%0)" ::"n"(n) : "memory")
#define WAITL0() asm volatile("s_waitcnt lgkmcnt(0)" ::: "memory")
#define BAR() asm volatile("s_waitcnt lgkmcnt(0)\n\ts_barrier" ::: "memory")
#define WAITBAR(n) asm volatile("s_waitcnt vmcnt(%0) lgkmcnt(0)\n\ts_barrier" ::"n"(n) : "memory")

template <int SC1>
DI void dma16(const void* base, u32 voff, u32 lds) {
    if constexpr (SC1) asm volatile("s_mov_b32 m0, %2\n\ts_nop 0\n\tglobal_load_lds_dwordx4 %0, %1 sc1" ::"v"(voff), "s"(base), "s"(lds) : "memory");
    else asm volatile("s_mov_b32 m0, %2\n\ts_nop 0\n\tglobal_load_lds_dwordx4 %0, %1" ::"v"(voff), "s"(base), "s"(lds) : "memory");
}
DI u32 ld32_sc1(const void* b, u32 o) { u32 v; asm volatile("global_load_dword %0, %1, %2 sc1" : "=v"(v) : "v"(o), "s"(b) : "memory"); return v; }
DI u32 ld16(const void* b, u32 o) { u32 v; asm volatile("global_load_ushort %0, %1, %2" : "=v"(v) : "v"(o), "s"(b) : "memory"); return v; }
DI u32x2 ld64_sc1(const void* b, u32 o) { u32x2 v; asm volatile("global_load_dwordx2 %0, %1, %2 sc1" : "=v"(v) : "v"(o), "s"(b) : "memory"); return v; }
DI u32x4 ld128_sc1(const void* b, u32 o) { u32x4 v; asm volatile("global_load_dwordx4 %0, %1, %2 sc1" : "=v"(v) : "v"(o), "s"(b) : "memory"); return v; }
DI void st32_sc1(void* b, u32 o, u32 v) { asm volatile("global_store_dword %0, %1, %2 sc1" ::"v"(o), "v"(v), "s"(b) : "memory"); }
DI void st64_sc1(void* b, u32 o, u32x2 v) { asm volatile("global_store_dwordx2 %0, %1, %2 sc1\n\ts_nop 1" ::"v"(o), "v"(v), "s"(b) : "memory"); }
DI void st128_sc1(void* b, u32 o, u32x4 v) { asm volatile("global_store_dwordx4 %0, %1, %2 sc1\n\ts_nop 1" ::"v"(o), "v"(v), "s"(b) : "memory"); }
template <int N>
DI u32 take32(u32 v) { u32 o; asm volatile("s_waitcnt vmcnt(%2)\n\tv_mov_b32 %0, %1" : "=v"(o) : "v"(v), "n"(N) : "memory"); return o; }

DI void poll32(const void* b, u32 o, u32 tag) {
#pragma unroll 1
    for (int it = 0; it < SPIN_CAP; ++it) {
        u32 v;
        asm volatile("global_load_dword %0, %1, %2 sc1\n\ts_waitcnt vmcnt(0)" : "=v"(v) : "v"(o), "s"(b) : "memory");
        if (wall(v == tag)) break;
        __builtin_amdgcn_s_sleep(16);
    }
}
DI void poll128(const void* b, u32 o, u32 tag) {
#pragma unroll 1
    for (int it = 0; it < SPIN_CAP; ++it) {
        u32x4 v;
        asm volatile("global_load_dwordx4 %0, %1, %2 sc1\n\ts_waitcnt vmcnt(0)" : "=v"(v) : "v"(o), "s"(b) : "memory");
        if (wall(v.x == tag && v.y == tag && v.z == tag && v.w == tag)) break;
        __builtin_amdgcn_s_sleep(16);
    }
}
DI bf16x8 trfrag(const char* a0, const char* a1) {
    s16x4 t0 = __builtin_amdgcn_ds_read_tr16_b64_v4i16((LDSP s16x4*)a0);
    s16x4 t1 = __builtin_amdgcn_ds_read_tr16_b64_v4i16((LDSP s16x4*)a1);
    return __builtin_bit_cast(bf16x8, __builtin_shufflevector(t0, t1, 0, 1, 2, 3, 4, 5, 6, 7));
}
DI f32x4 mfma(bf16x8 a, bf16x8 b, f32x4 c) { return __builtin_amdgcn_mfma_f32_16x16x32_bf16(a, b, c, 0, 0, 0); }
DI f32x4 mfmah(f16x8 a, f16x8 b, f32x4 c) { return __builtin_amdgcn_mfma_f32_16x16x32_f16(a, b, c, 0, 0, 0); }

struct Cplx { float r, i; };
DI Cplx cmul(Cplx a, Cplx b) { return {__builtin_fmaf(a.r, b.r, -(a.i * b.i)), __builtin_fmaf(a.r, b.i, a.i * b.r)}; }
DI Cplx csq(Cplx a) { return {__builtin_fmaf(a.r, a.r, -(a.i * a.i)), (a.r + a.r) * a.i}; }

constexpr int g1_ops(int n) { return ((n >= 1 && n <= 16) ? 1 : 0) + ((n >= 2 && n <= 15) ? 1 : 0); }
constexpr int g1_allow(int n) {
    if (n == 0) return 7;
    if (n == 1) return 6;
    if (n == 2) return 3 + g1_ops(1);
    if (n == 3) return 2 + g1_ops(1) + g1_ops(2);
    if ((n & 1) == 0 && n >= 6) return g1_ops(n - 2) + g1_ops(n - 1);
    return (g1_ops(n - 3) - 1) + g1_ops(n - 2) + g1_ops(n - 1);
}
constexpr int g2_hi(int i) { int m = (10 * (i - 1) + 64) / 8; if (m < 8) m = 8; return m > 34 ? 34 : m; }
constexpr int g2_lo(int i) { return i == 0 ? 8 : g2_hi(i - 1); }
constexpr int g2_ops(int i, int AD) { return (g2_hi(i) - g2_lo(i)) + ((i + AD < 27) ? 1 : 0) + (i == 20 ? 2 : 0) + (i == 23 ? 1 : 0); }
constexpr int g2_allow(int I, int AD) {
    int a = 0;
    if (I < AD) { a = AD - 1 - I; for (int i = 0; i < I; ++i) a += g2_ops(i, AD); }
    else { a = g2_ops(I - AD, AD) - (g2_hi(I - AD) - g2_lo(I - AD)) - 1; for (int i = I - AD + 1; i < I; ++i) a += g2_ops(i, AD); }
    const int need = (10 * I + 9) / 8;
    if (need >= 8) {
        int it = 0;
        while (g2_hi(it) <= need) ++it;
        int b = g2_ops(it, AD) - (need - g2_lo(it)) - 1;
        for (int i = it + 1; i < I; ++i) b += g2_ops(i, AD);
        if (b < a) a = b;
    }
    return a;
}
#define SB() __builtin_amdgcn_sched_barrier(0)

__global__ __launch_bounds__(512, 4) void fused_kernel(
    const u16* __restrict__ x, const u16* __restrict__ Are, const u16* __restrict__ Aim, const u16* __restrict__ Bre,
    const u16* __restrict__ Bim, const u16* __restrict__ Cre, const u16* __restrict__ Cim, const u16* __restrict__ logdt,
    const u16* __restrict__ Dsk, const u16* __restrict__ w1, const u16* __restrict__ b1, const u16* __restrict__ w2,
    u16* __restrict__ out, char* __restrict__ ws) {
    extern __shared__ __attribute__((aligned(16))) char smem[];
    const int tid = __builtin_amdgcn_workitem_id_x();
    const int l = tid & 63;
    const int w = __builtin_amdgcn_readfirstlane(tid >> 6);
    const int g = l >> 4, c16 = l & 15;
    const int bid = __builtin_amdgcn_workgroup_id_x();
    const u32 lb = (u32)(size_t)(LDSP char*)smem;

    auto w2_load = [&](int ks, u32x4 (&wv)[5]) {
#pragma unroll
        for (int i = 0; i < 5; ++i) {
            const int n = i * 64 + l;
            wv[i] = *(const u32x4*)((const char*)w2 + (u32)((ks * 32 + n / 10) * 1280 + w * 160 + (n % 10) * 16));
        }
    };
    auto w2_store = [&](int ks, const u32x4 (&wv)[5]) {
        char* tile = smem + w * 5632;
#pragma unroll
        for (int i = 0; i < 5; ++i) {
            const int n = i * 64 + l;
            *(u32x4*)(tile + (n / 10) * 176 + (n % 10) * 16) = wv[i];
        }
#pragma unroll
        for (int dd = 0; dd < 5; ++dd) {
            const char* a = tile + (4 * g + (c16 >> 2)) * 176 + dd * 32 + 8 * (l & 3);
            const bf16x8 f = trfrag(a, a + 16 * 176);
            st128_sc1(ws, WS_W2T + (u32)(((w * 54 + ks) * 5 + dd) * 1024 + l * 16), __builtin_bit_cast(u32x4, f));
        }
    };

    const bool isG = bid >= 256;
    const int gb = bid & 255;
    const int xcd = gb & 7, idx = gb >> 3;
    const int tk = w >> 2, ch = (w >> 1) & 1, p = w & 1;
    const bool doconv = isG ? (idx >= 28 && tk == 0) : (tk == 0 || idx >= 28);
    const int d0 = 80 * xcd + 2 * (isG ? idx + 8 : idx + 4 * tk);
    const int d = d0 + ch;
    u32 pa[6] = {0, 0, 0, 0, 0, 0}, pld = 0, pdk = 0;
    auto load_params = [&]() {
        const u32 pi = (u32)(d * NM + p * 64 + l);
        pa[0] = Are[pi]; pa[1] = Aim[pi]; pa[2] = Bre[pi]; pa[3] = Bim[pi]; pa[4] = Cre[pi]; pa[5] = Cim[pi];
        pld = logdt[d]; pdk = Dsk[d];
    };
    u32 tag = 0;
    bool pfd = false;
    const int t2 = 8 * (xcd >> 1) + (idx & 7), cg = 4 * (xcd & 1) + (idx >> 3);
    const int r32 = l & 31, pw = w & 3;
    const u32 gw2 = WS_W2T + (u32)(cg * 54 * 5120 + l * 16);

    if (isG) {
        __builtin_amdgcn_s_setprio(2);
        const bool has1 = idx < 28;
        const int tt = 4 * (xcd >> 1) + (idx & 3);
        const int ct = 7 * (xcd & 1) + (idx >> 2);
        const bool half = (ct == 13);
        float* csum = (float*)(smem + 73728);

        const int th = pw & 1, fh = pw >> 1, tbe = w >> 2;
        auto epilogue1 = [&](const auto& a) {
            const u32* sp = (const u32*)(csum + 384) + 64 * th + 32 * tbe + c16;
            const u32 s0 = sp[0], s1 = sp[16];
            const float nmt[2] = {-lo16(s0), -lo16(s1)};
            const float rt[2] = {1.44269504f * __builtin_amdgcn_rcpf(hi16(s0)), 1.44269504f * __builtin_amdgcn_rcpf(hi16(s1))};
#pragma unroll
            for (int kk = 0; kk < 2; ++kk) {
                const float* cp = csum + fh * 64 + (kk ^ th) * 32 + 4 * g;
                const u32 abase = WS_ACT + (u32)(((tt * 8 + 4 * th + 2 * tbe) * 54 + ct * 4 + fh * 2 + (kk ^ th)) * 1024 + l * 16);
                const f32x4 c4[2] = {*(const f32x4*)(cp), *(const f32x4*)(cp + 16)};
                const f32x4 b4[2] = {*(const f32x4*)(cp + 128), *(const f32x4*)(cp + 144)};
#pragma unroll
                for (int tb = 0; tb < 2; ++tb) {
                    u32 pk[4];
#pragma unroll
                    for (int e2 = 0; e2 < 2; ++e2) {
                        float r[4];
#pragma unroll
                        for (int i = 0; i < 4; ++i) {
                            const float h2 = __builtin_fmaf(__builtin_fmaf(nmt[tb], c4[e2][i], a[tb][2 * kk + e2][i]), rt[tb], b4[e2][i]);
                            const float e = __builtin_amdgcn_exp2f(h2);
                            const float rr = __builtin_amdgcn_rcpf(__builtin_fmaf(e, e + 2.f, 2.f));
                            r[i] = h2 * __builtin_fmaf(-1.38629436f, rr, 0.69314718f);
                        }
                        pk[2 * e2] = pk2(r[0], r[1]);
                        pk[2 * e2 + 1] = pk2(r[2], r[3]);
                    }
                    st128_sc1(ws, abase + tb * (54 * 1024), u32x4{pk[0], pk[1], pk[2], pk[3]});
                }
            }
            WAITV(0);
            if (l == 0) st32_sc1(ws, WS_ACTF + (u32)(((tt * 4 + 2 * th + tbe) * 32 + ct * 2 + fh) * 4), tag);
        };
        char* hand = smem + 32768 + pw * 2048 + l * 16;
        const bool eact = !(half && fh == 1);

        if (w >= 4) {
            const int lw = w - 4;
            const u32 ev = ld32_sc1(ws, WS_EPOCH);
            if (has1) {
                u32 gx[2], gw[2];
#pragma unroll
                for (int e = 0; e < 2; ++e) {
                    const int vw = 2 * lw + e;
                    gx[e] = (u32)((tt * 128 + 8 * vw + (l >> 3)) * 1280 + (((l & 7) ^ ((4 * e + g) & 7)) << 4));
                    int wch = c16 ^ ((g << 2) | (vw & 3));
                    if (half) wch &= 7;
                    gw[e] = (u32)((4 * vw + g) * 3456 + ct * 256 + (wch << 4));
                }
                const u32 ldst = lb + lw * 2048;
                auto issueW = [&](auto NC) {
                    constexpr int N = decltype(NC)::value;
                    constexpr u32 o = N * (32 * 3456);
                    dma16<0>(w1, gw[0] + o, ldst + (N & 3) * 8192);
                    dma16<0>(w1, gw[1] + o, ldst + (N & 3) * 8192 + 1024);
                };
                auto issueX = [&](auto JC, auto UC) {
                    constexpr int j = decltype(JC)::value, U = decltype(UC)::value;
                    constexpr u32 o = U * (64 * 1280) + j * 128;
                    dma16<0>(x, gx[0] + o, ldst + 32768 + (j % 3) * 16384 + U * 8192);
                    dma16<0>(x, gx[1] + o, ldst + 32768 + (j % 3) * 16384 + U * 8192 + 1024);
                };
                issueW(IC<0>{}); issueX(IC<0>{}, IC<0>{}); issueX(IC<0>{}, IC<1>{}); issueW(IC<1>{}); issueW(IC<2>{});
                issueX(IC<1>{}, IC<0>{}); issueX(IC<1>{}, IC<1>{}); issueW(IC<3>{}); issueX(IC<2>{}, IC<0>{}); issueX(IC<2>{}, IC<1>{});
                u32 bv = 0, w2flag = 0;
                u32x2 st0;
                const u32 stoff = WS_STAT + (u32)((tt * 128 + (tid & 127)) * 8);
                auto lstep = [&](auto NC) {
                    constexpr int N = decltype(NC)::value;
                    WAITBAR(2 * g1_allow(N) + (N >= 18 ? 3 : 0));
                    if constexpr (N >= 1 && N <= 16) issueW(IC<N + 3>{});
                    if constexpr (N >= 2 && N <= 15) issueX(IC<N / 2 + 2>{}, IC<N & 1>{});
                    if constexpr (N == 0) {
                        tag = mktag(take32<63>(ev));
                        if (tid == 256) st32_sc1(ws, WS_RD + bid * 4, tag);
                    }
                    if constexpr (N == 17) {
                        bv = ld16(b1, (u32)((ct * 128 + (tid & (half ? 63 : 127))) * 2));
                        st0 = ld64_sc1(ws, stoff);
                        w2flag = ld32_sc1(ws, WS_W2F + (u32)(cg * 256 + (l < 54 ? l : 0) * 4));
                    }
                };
                lstep(IC<0>{}); lstep(IC<1>{}); lstep(IC<2>{}); lstep(IC<3>{}); lstep(IC<4>{}); lstep(IC<5>{}); lstep(IC<6>{});
                lstep(IC<7>{}); lstep(IC<8>{}); lstep(IC<9>{}); lstep(IC<10>{}); lstep(IC<11>{}); lstep(IC<12>{}); lstep(IC<13>{});
                lstep(IC<14>{}); lstep(IC<15>{}); lstep(IC<16>{}); lstep(IC<17>{}); lstep(IC<18>{}); lstep(IC<19>{});
                asm volatile("s_waitcnt vmcnt(0)\n\ts_barrier" : "+v"(bv), "+v"(st0), "+v"(w2flag)::"memory");
                if (tid < 384) csum[128 + (tid & 127)] = bf2f(bv) * 1.44269504f;
                if (tid == 384) csum[256] = asf(tag);
                if (tid >= 384) {
#pragma unroll 1
                    for (int it = 0; it < SPIN_CAP; ++it) {
                        if (wall(st0.y == tag)) break;
                        asm volatile("global_load_dwordx2 %0, %1, %2 sc1\n\ts_waitcnt vmcnt(0)" : "=v"(st0) : "v"(stoff), "s"(ws) : "memory");
                    }
                    ((u32*)csum)[384 + (tid & 127)] = st0.x;
                }
                BAR();
                pfd = wall(w2flag == tag);
                if (eact) {
                    f32x4 a[2][4];
#pragma unroll
                    for (int i = 0; i < 8; ++i) a[i >> 2][i & 3] = *(const f32x4*)(hand + (i >> 1) * 8192 + (i & 1) * 1024);
                    WAITL0();
                    epilogue1(a);
                }
            } else {
                tag = mktag(take32<0>(ev));
            }
        } else if (has1) {
            int xa[2];
            xa[0] = 32768 + th * 8192 + c16 * 128 + ((g ^ (c16 >> 1)) << 4);
            xa[1] = xa[0] ^ 64;
            asm volatile("" : "+v"(xa[0]), "+v"(xa[1]));
            int wa[2];
            {
                const int q = c16 >> 2, p = l & 3;
#pragma unroll
                for (int t = 0; t < 2; ++t) {
                    const int row = 8 * g + 4 * t + q;
                    const int f = (q << 2) | ((2 * g + t) & 3);
                    wa[t] = (row * 256 + (((p >> 1) ^ f) << 4) + 8 * (p & 1)) ^ (fh << 7) ^ (th << 6);
                }
            }
            f32x4 acc[4][4];
#pragma unroll
            for (int i = 0; i < 16; ++i) acc[i >> 2][i & 3] = f32x4{0.f, 0.f, 0.f, 0.f};
            float cs[2] = {0.f, 0.f};
            const bf16x2 ones = __builtin_bit_cast(bf16x2, 0x3F803F80u);
            bf16x8 xf[4], wf[4];
            auto rdx = [&](auto NC, int tb) {
                constexpr int N = decltype(NC)::value;
                if constexpr (N < 20) xf[tb] = *(const bf16x8*)(smem + ((N >> 1) % 3) * 16384 + tb * 2048 + xa[N & 1]);
            };
            auto rdw = [&](auto NC, int a) {
                constexpr int N = decltype(NC)::value;
                if constexpr (N < 20) wf[a] = trfrag(smem + (N & 3) * 8192 + (wa[0] ^ (a << 5)), smem + (N & 3) * 8192 + (wa[1] ^ (a << 5)));
            };
            auto dots = [&](int a) {
                const u32x4 wv = __builtin_bit_cast(u32x4, wf[a]);
#pragma unroll
                for (int e = 0; e < 4; ++e) {
                    const u32 we = wv[e];
                    cs[a] = __builtin_amdgcn_fdot2_f32_bf16(__builtin_bit_cast(bf16x2, we), ones, cs[a], false);
                }
                asm volatile("" : "+v"(cs[a]));
            };
            auto cstep = [&](auto NC) {
                constexpr int N = decltype(NC)::value;
#pragma unroll
                for (int tb = 0; tb < 4; ++tb) {
                    acc[tb][0] = mfma(wf[0], xf[tb], acc[tb][0]);
                    acc[tb][1] = mfma(wf[1], xf[tb], acc[tb][1]);
                    SB();
                    if (tb < 2) rdw(NC, 2 + tb); else dots(tb - 2);
                    SB();
                }
                SB();
                BAR();
#pragma unroll
                for (int tb = 0; tb < 4; ++tb) {
                    acc[tb][2] = mfma(wf[2], xf[tb], acc[tb][2]);
                    acc[tb][3] = mfma(wf[3], xf[tb], acc[tb][3]);
                    SB();
                    rdx(IC<N + 1>{}, tb);
                    if (tb < 2) rdw(IC<N + 1>{}, tb);
                    SB();
                }
            };
            BAR();
            rdx(IC<0>{}, 0); rdw(IC<0>{}, 0); rdw(IC<0>{}, 1); rdx(IC<0>{}, 1); rdx(IC<0>{}, 2); rdx(IC<0>{}, 3);
#define C4(m) cstep(IC<m>{}); cstep(IC<m + 1>{}); cstep(IC<m + 2>{}); cstep(IC<m + 3>{});
            C4(0) C4(4) C4(8) C4(12) C4(16)
#undef C4
#pragma unroll
            for (int i = 0; i < 8; ++i) *(f32x4*)(hand + (i >> 1) * 8192 + (i & 1) * 1024) = acc[2 + (i >> 2)][i & 3];
#pragma unroll
            for (int a = 0; a < 2; ++a) {
                float v = cs[a];
                v += asf((u32)__builtin_amdgcn_ds_bpermute((l ^ 16) * 4, (int)asu(v)));
                v += asf((u32)__builtin_amdgcn_ds_bpermute((l ^ 32) * 4, (int)asu(v)));
                if (l < 16) csum[fh * 64 + th * 32 + a * 16 + l] = v;
            }
            BAR();
            tag = asu(csum[256]);
            if (eact) epilogue1(acc);
        }
        if (idx >= 28) {
            if (w < 4) { const u32 ev = ld32_sc1(ws, WS_EPOCH); tag = mktag(take32<0>(ev)); }
            if (doconv) load_params();
            {
                const int k0 = gb - 224;
                u32x4 wv[5], wv2[5];
                w2_load(k0, wv);
                if (k0 < 22) w2_load(k0 + 32, wv2);
                w2_store(k0, wv);
                if (k0 < 22) w2_store(k0 + 32, wv2);
                WAITV(0);
                if (l == 0) st32_sc1(ws, WS_W2F + (u32)((w * 64 + k0) * 4), tag);
                if (k0 < 22 && l == 0) st32_sc1(ws, WS_W2F + (u32)((w * 64 + k0 + 32) * 4), tag);
            }
            BAR();
            if (tid == 0) st32_sc1(ws, WS_RD + bid * 4, tag);
        }

    } else {
    const int tg = 64 * (xcd >> 1) + 32 * (xcd & 1) + idx;
    u32 xv[5];
    {
        const u32* xr = (const u32*)x + (u32)((tg * 8 + w) * 320 + l);
#pragma unroll
        for (int i = 0; i < 5; ++i) xv[i] = xr[64 * i];
    }
    const u32 ev = ld32_sc1(ws, WS_EPOCH);
    if (doconv) load_params();
    char* lnt = smem + 65536;
    float mb = 0.f, den = 1.f;
    {
        float s = 0.f;
#pragma unroll
        for (int i = 0; i < 5; ++i) s += lo16(xv[i]) + hi16(xv[i]);
        const float mean = wave_sum(s) * (1.f / 640.f);
        float q = 0.f;
#pragma unroll
        for (int i = 0; i < 5; ++i) {
            const float a = lo16(xv[i]) - mean, b = hi16(xv[i]) - mean;
            q = __builtin_fmaf(a, a, q); q = __builtin_fmaf(b, b, q);
        }
        const float var = wave_sum(q) * (1.f / 640.f);
        mb = rbf(mean);
        den = rbf(__builtin_sqrtf(rbf(rbf(var) + 1e-5f)));
        const float rden = 1.0f / den;
        char* lw = lnt + w * 1280 + l * 4 + w * 64;
#pragma unroll
        for (int i = 0; i < 5; ++i) {
            const u32 dd = pk2(lo16(xv[i]) - mb, hi16(xv[i]) - mb);
            const u32 o = pk2(lo16(dd) * rden, hi16(dd) * rden);
            if (i < 3) *(u32*)(lw + 256 * i) = o;
            else *(u32*)(lw + 256 * i - ((256 * i + l * 4 + w * 64 >= 1280) ? 1280 : 0)) = o;
        }
    }
    tag = mktag(doconv ? take32<8>(ev) : take32<0>(ev));
    if (l == 0) st64_sc1(ws, WS_STAT + (u32)((tg * 8 + w) * 8), u32x2{pk2(mb, den), tag});
    BAR();
    if (tid == 0) st32_sc1(ws, WS_RD + bid * 4, tag);
    {
        const int q = c16 >> 2;
        auto piece = [&](int i) {
            int p0 = 2 * (i * 4 + g) + 4 * q, p1 = p0 + 16;
            if (p0 >= 80) p0 -= 80;
            if (p1 >= 80) p1 -= 80;
            const bf16x8 f = trfrag(lnt + q * 1280 + p0 * 16 + 8 * (l & 3), lnt + (q + 4) * 1280 + p1 * 16 + 8 * (l & 3));
            st128_sc1(ws, WS_XB + (u32)(tg * 10240 + i * 1024 + l * 16), __builtin_bit_cast(u32x4, f));
        };
        piece(w);
        if (w >= 6) piece(2 + w);
    }
    if (!doconv) {
        WAITBAR(0);
        if (bid == 0 && w == 7) {
            poll128(ws, WS_RD + l * 16, tag);
            poll128(ws, WS_RD + 1024 + l * 16, tag);
            if (l == 0) st32_sc1(ws, WS_EPOCH, tag);
        }
        return;
    }
    }
    if (doconv) {
        const float dt = rbf(__builtin_expf(bf2f(pld)));
        const float lr2 = (dt * bf2f(pa[0])) * 1.44269504f;
        const float li = dt * bf2f(pa[1]);
        const float rh = li * 0.15915494f;
        const float rl = __builtin_fmaf(li, 0.15915494f, -rh) + li * 3.0908620e-9f;
        auto E = [&](float t) {
            const float ph = rh * t;
            const float er = __builtin_fmaf(rl, t, __builtin_fmaf(rh, t, -ph));
            const float fr = (ph - __builtin_rintf(ph)) + er;
            const float m = __builtin_amdgcn_exp2f(lr2 * t);
            return Cplx{m * __builtin_amdgcn_cosf(fr), m * __builtin_amdgcn_sinf(fr)};
        };
        char* scr = smem + w * 4608;
        auto put = [&](int row, Cplx v) { *(u32*)(scr + row * 288 + l * 4) = pkh(v.r, v.i); };
        const char* frd = scr + c16 * 288 + g * 16;
        f16x8 bq[4];
        float inv_scale;
        {
            Cplx c = cmul(Cplx{bf2f(pa[4]), bf2f(pa[5])}, Cplx{dt * bf2f(pa[2]), dt * bf2f(pa[3])});
            const float mx = wave_max(__builtin_fmaxf(__builtin_fabsf(c.r), __builtin_fabsf(c.i)));
            int ex = 14 - __builtin_amdgcn_frexp_expf(mx);
            ex = ex > 100 ? 100 : (ex < -100 ? -100 : ex);
            inv_scale = __builtin_amdgcn_ldexpf(1.f, -ex);
            c.r = __builtin_amdgcn_ldexpf(c.r, ex);
            c.i = -__builtin_amdgcn_ldexpf(c.i, ex);
            Cplx e8 = E(8.f);
            e8.i = -e8.i;
            const Cplx e16 = csq(e8), e32 = csq(e16), e64 = csq(e32);
            Cplx b[8];
            b[0] = c; b[1] = cmul(c, e8); b[2] = cmul(c, e16); b[3] = cmul(b[1], e16);
#pragma unroll
            for (int j = 0; j < 4; ++j) b[4 + j] = cmul(b[j], e32);
#pragma unroll
            for (int j = 0; j < 8; ++j) { put(j, b[j]); put(8 + j, cmul(b[j], e64)); }
#pragma unroll
            for (int s = 0; s < 4; ++s) bq[s] = *(const f16x8*)(frd + s * 64);
        }
        WAITBAR(0);
        if (tid == 0 && !isG) st32_sc1(ws, WS_XBF + gb * 4, tag);
        u32 ix[9];
#pragma unroll
        for (int k = 0; k < 9; ++k) ix[k] = g_idx.v[k * 128 + p * 64 + l];
        auto tile_mm = [&]() {
            f32x4 c = {0, 0, 0, 0};
#pragma unroll
            for (int s = 0; s < 4; ++s) c = mfmah(*(const f16x8*)(frd + s * 64), bq[s], c);
            return c;
        };
        f32x4 T0, T1, T2;
        {
            const Cplx e1 = E(1.f), e2 = csq(e1), e3 = cmul(e2, e1), e4 = csq(e2), e5 = cmul(e4, e1), e6 = cmul(e4, e2), e7 = cmul(e4, e3);
            const Cplx e128 = E(128.f);
            put(0, Cplx{1.f, 0.f}); put(1, e1); put(2, e2); put(3, e3); put(4, e4); put(5, e5); put(6, e6); put(7, e7);
            put(8, e128); put(9, cmul(e128, e1)); put(10, cmul(e128, e2)); put(11, cmul(e128, e3));
            put(12, cmul(e128, e4)); put(13, cmul(e128, e5)); put(14, cmul(e128, e6)); put(15, cmul(e128, e7));
            T0 = tile_mm();
            const Cplx e256 = csq(e128), e384 = cmul(e256, e128), e512 = csq(e256);
            const Cplx e640 = cmul(e512, e128), e768 = cmul(e512, e256), e896 = cmul(e512, e384);
            put(0, e256); put(1, cmul(e256, e2)); put(2, cmul(e256, e4)); put(3, cmul(e256, e6));
            put(4, e384); put(5, cmul(e384, e2)); put(6, cmul(e384, e4)); put(7, cmul(e384, e6));
            put(8, e512); put(9, cmul(e512, e4)); put(10, e640); put(11, cmul(e640, e4));
            put(12, e768); put(13, cmul(e768, e4)); put(14, e896); put(15, cmul(e896, e4));
            T1 = tile_mm();
            const Cplx e1k = E(1024.f);
            put(0, e1k); put(1, cmul(e1k, e128)); put(2, cmul(e1k, e256)); put(3, cmul(e1k, e384)); put(4, cmul(e1k, e512));
            put(5, cmul(e1k, e640)); put(6, cmul(e1k, e768)); put(7, cmul(e1k, e896)); put(8, csq(e1k));
            T2 = tile_mm();
        }
        {
            float* tabp = (float*)scr;
            const int j = c16;
            *(f32x4*)(tabp + 128 * (g >> 1) + 4 * (g & 1) + 8 * j) = T0 * inv_scale;
            T1 *= inv_scale;
            T2 *= inv_scale;
            if (g < 2) {
                *(f32x4*)(tabp + 256 + 64 * g + 4 * j) = T1;
#pragma unroll
                for (int i = 0; i < 4; ++i) tabp[512 + 16 * (4 * g + i) + j] = T2[i];
            } else {
                *(f32x2*)(tabp + 384 + 64 * (g - 2) + 2 * j) = f32x2{T1[0], T1[1]};
                *(f32x2*)(tabp + 384 + 64 * (g - 2) + 32 + 2 * j) = f32x2{T1[2], T1[3]};
                if (g == 2) tabp[640 + j] = T2[0];
            }
        }
        if (w == 0) poll128(ws, WS_XBF + l * 16, tag);
        BAR();
        char* U = smem + 36864 + tk * 9216;
#pragma unroll
        for (int i = 0; i < 2; ++i) {
            const int c = ((w & 3) * 2 + i) * 32 + (l >> 1);
            const int chs = (l & 1) ^ ((c >> 3) & 1);
            dma16<1>(ws, WS_XB + (u32)(c * 10240 + (d0 + chs) * 16), lb + 36864 + tk * 9216 + 1024 + ((w & 3) * 2 + i) * 1024);
        }
        if ((w & 3) == 0) *(u32x4*)(U + l * 16) = u32x4{0, 0, 0, 0};
        char* tab = smem + 76288 + (w >> 1) * 1344;
        {
            const float* t0 = (const float*)(smem + (w & 6) * 4608);
            const float* t1 = (const float*)(smem + (w | 1) * 4608);
#pragma unroll
            for (int it = 0; it < 3; ++it) {
                const int i = it * 128 + p * 64 + l;
                if (i < 328) {
                    const f32x2 a = *(const f32x2*)(t0 + 2 * i), b = *(const f32x2*)(t1 + 2 * i);
                    *(u32*)(tab + 4 * i) = pk2(a.x + b.x, a.y + b.y);
                } else if (i == 330) *(u32*)(tab + 4 * i) = 0u;
            }
        }
        BAR();
        char* KE = smem + (w >> 1) * 8448;
        char* KO = KE + 4160;
        {
            const int m = p * 64 + l;
            u32 e[9];
#pragma unroll
            for (int k = 0; k < 9; ++k) {
                u16x2 v;
                v.x = *(const u16*)(tab + (ix[k] & 0xFFFFu));
                v.y = *(const u16*)(tab + (ix[k] >> 16));
                e[k] = __builtin_bit_cast(u32, v);
            }
            *(u32x4*)(KE + m * 32) = u32x4{e[0], e[1], e[2], e[3]};
            *(u32x4*)(KE + m * 32 + 16) = u32x4{e[4], e[5], e[6], e[7]};
            u32 o[8];
#pragma unroll
            for (int k = 0; k < 8; ++k) o[k] = __builtin_amdgcn_alignbit(e[k + 1], e[k], 16);
            *(u32x4*)(KO + m * 32) = u32x4{o[0], o[1], o[2], o[3]};
            *(u32x4*)(KO + m * 32 + 16) = u32x4{o[4], o[5], o[6], o[7]};
            if (p == 0 && l < 4) *(u32x4*)(KE + 4096 + (l & 1) * 16 + (l >> 1) * 4160) = u32x4{0, 0, 0, 0};
        }
        WAITBAR(0);
        f32x4 y[8];
#pragma unroll
        for (int a = 0; a < 8; ++a) y[a] = f32x4{0, 0, 0, 0};
#pragma unroll 1
        for (int rr = 0; rr < 4; ++rr) {
            const int r = 8 * p + 2 * rr;
            const int c = 2 * (c16 - r - 1) + g + 32;
            const char* ub = U + c * 32 + ((ch ^ ((c >> 3) & 1)) << 4);
            bf16x8 B[8];
#pragma unroll
            for (int a = 0; a < 8; ++a) B[a] = *(const bf16x8*)(ub + a * 1024);
            const int i0 = 2031 - 16 * r - c16 + 8 * g;
            const char* kb = ((i0 & 1) ? KO : KE) + (i0 >> 1) * 4;
#pragma unroll
            for (int q = 0; q < 8; ++q) {
                const u32x4a av = *(const u32x4a*)(kb - q * 512);
                const bf16x8 A = __builtin_bit_cast(bf16x8, av);
#pragma unroll
                for (int a = q; a < 8; ++a) y[a] = mfma(A, B[a - q], y[a]);
            }
        }
        if (!isG) __builtin_amdgcn_s_setprio(3);
        const float dsk = bf2f(pdk);
        BAR();
        char* ew = smem + w * 4096 + l * 16;
        const char* eo = smem + (w ^ 1) * 4096 + l * 16;
        char* so = smem + 55296 + w * 2048;
        auto fin = [&](int a, f32x4 yy) {
            yy += *(const f32x4*)(eo + a * 1024);
            const int cu = 2 * (16 * (4 * p + a) + c16) + (g >> 1) + 32;
            const u32x2 uv = *(const u32x2*)(U + cu * 32 + ((ch ^ ((cu >> 3) & 1)) << 4) + (g & 1) * 8);
            *(u32x2*)(so + a * 512 + c16 * 32 + g * 8) =
                u32x2{pk2(__builtin_fmaf(lo16(uv.x), dsk, yy[0]), __builtin_fmaf(hi16(uv.x), dsk, yy[1])),
                      pk2(__builtin_fmaf(lo16(uv.y), dsk, yy[2]), __builtin_fmaf(hi16(uv.y), dsk, yy[3]))};
        };
        if (p == 0) {
#pragma unroll
            for (int a = 0; a < 4; ++a) *(f32x4*)(ew + a * 1024) = y[4 + a];
            BAR();
#pragma unroll
            for (int a = 0; a < 4; ++a) fin(a, y[a]);
        } else {
#pragma unroll
            for (int a = 0; a < 4; ++a) *(f32x4*)(ew + a * 1024) = y[a];
            BAR();
#pragma unroll
            for (int a = 0; a < 4; ++a) fin(a, y[4 + a]);
        }
#pragma unroll
        for (int i = 0; i < 2; ++i) {
            const u32x4 v = *(const u32x4*)(so + i * 1024 + l * 16);
            st128_sc1(ws, WS_ST + (u32)(d * 4096 + p * 2048 + i * 1024 + l * 16), v);
        }
        WAITV(0);
        if (l == 0) st32_sc1(ws, WS_SF + (u32)((p * 640 + d) * 4), tag);
    } else if (isG && idx >= 28) {
        BAR(); BAR(); BAR(); BAR(); BAR(); BAR();
    }
    if (!isG) return;
    {
        {
            const int tbk = w & 3, kp = w >> 2;
            BAR();
            if (w >= 4) {
                if (!pfd) poll32(ws, WS_W2F + (u32)(cg * 256 + (l < 54 ? l : 0) * 4), tag);
#pragma unroll
                for (int m = 0; m < 16; ++m)
                    dma16<1>(ws, gw2 + ((m >> 1) * 8 + (m & 1) + 2 * pw) * 1024, lb + ((m >> 1) * 8 + (m & 1) + 2 * pw) * 1024);
            }
            auto issue_w2 = [&](auto MC) {
                constexpr int M = decltype(MC)::value;
                if (M < 33 || w < 6) dma16<1>(ws, gw2 + (8 * M + w) * 1024, lb + (M & 7) * 8192 + w * 1024);
                else dma16<1>(ws, gw2, lb + 73728 + 2048 + (w & 1) * 1024);
            };
            u32 rmask = 0;
            const u32 afo = WS_ACTF + (u32)(((t2 * 2 + (tbk >> 1)) * 32 + r32) * 4);
            auto need = [&](int bit) {
                if (__builtin_expect(!((rmask >> bit) & 1u), 0)) {
#pragma unroll 1
                    for (int it = 0; it < SPIN_CAP; ++it) {
                        u32 v;
                        asm volatile("global_load_dword %0, %1, %2 sc1\n\ts_waitcnt vmcnt(0)" : "=v"(v) : "v"(afo), "s"(ws) : "memory");
                        rmask = (u32)__builtin_amdgcn_ballot_w64(v == tag);
                        if ((rmask >> bit) & 1u) break;
                        __builtin_amdgcn_s_sleep(8);
                    }
                }
            };
            const u32 ga = WS_ACT + (u32)(((4 * t2 + tbk) * 54 + kp) * 1024 + l * 16);
            constexpr int AD = 6;
            u32x4 af[8];
#pragma unroll
            for (int i = 0; i < AD; ++i) { need(i); af[i] = ld128_sc1(ws, ga + i * 2048); }
            f32x4 a2[5];
#pragma unroll
            for (int d = 0; d < 5; ++d) a2[d] = f32x4{0, 0, 0, 0};
            const int wl = kp * 5120 + l * 16;
            u32 sf0 = 0, sf1 = 0;
            bool sdone = false;
            const int n0 = w * 64 + l, n1 = 512 + (w & 1) * 64 + l;
            const u32 gx0 = (u32)((t2 * 64 + n0 / 10) * 1280 + cg * 160 + (n0 % 10) * 16);
            const u32 gx1 = (u32)((t2 * 64 + n1 / 10) * 1280 + cg * 160 + (n1 % 10) * 16);
            const u32 gs0 = WS_ST + (u32)((cg * 80 + (n0 >> 3)) * 4096 + t2 * 128 + (n0 & 7) * 16);
            const u32 gs1 = WS_ST + (u32)((cg * 80 + (n1 >> 3)) * 4096 + t2 * 128 + (n1 & 7) * 16);
            constexpr int XS = 16384, SS = 65536;
            const u32 sfp = WS_SF + (u32)(((t2 >> 4) * 640 + cg * 80) * 4);
            bf16x8 wq[2][5];
            auto load_s = [&]() {
                dma16<1>(ws, gs0, lb + SS + w * 1024);
                if (w < 2) dma16<1>(ws, gs1, lb + SS + 8192 + w * 1024);
            };
            auto st2 = [&](auto IC_) {
                constexpr int I = decltype(IC_)::value, B = I & 1;
                constexpr int allow = g2_allow(I, AD);
                SB();
                if constexpr (I == 24)
                    asm volatile("s_waitcnt vmcnt(%3)\n\ts_barrier" : "+v"(af[I & 7]), "+v"(sf0), "+v"(sf1) : "n"(allow < g2_ops(21, AD) + g2_ops(22, AD) + g2_ops(23, AD) ? allow : g2_ops(21, AD) + g2_ops(22, AD) + g2_ops(23, AD)) : "memory");
                else
                    asm volatile("s_waitcnt vmcnt(%1)\n\ts_barrier" : "+v"(af[I & 7]) : "n"(allow) : "memory");
                const bf16x8 bfrag = __builtin_bit_cast(bf16x8, af[(I + 7) & 7]);
#pragma unroll
                for (int d = 0; d < 5; ++d) {
                    if constexpr (I >= 1) a2[d] = mfma(wq[B ^ 1][d], bfrag, a2[d]);
                    SB();
                    {
                        const int o0 = ((10 * I + d) & 63) * 1024;
                        if (o0 + 5120 + 1024 <= 65536) wq[B][d] = *(const bf16x8*)(smem + wl + o0);
                        else wq[B][d] = *(const bf16x8*)(smem + ((wl + o0) & 65535));
                    }
                    if (d == 0) { if constexpr (g2_lo(I) < g2_hi(I)) issue_w2(IC<g2_lo(I)>{}); }
                    if (d == 1) { if constexpr (g2_lo(I) + 1 < g2_hi(I)) issue_w2(IC<g2_lo(I) + 1>{}); }
                    if (d == 2) { if constexpr (I + AD < 27) { need(I + AD); af[(I + AD) & 7] = ld128_sc1(ws, ga + (I + AD) * 2048); } }
                    SB();
                }
                if constexpr (I == 20) {
                    sf0 = ld32_sc1(ws, sfp + l * 4);
                    sf1 = ld32_sc1(ws, sfp + 256 + c16 * 4);
                }
                if constexpr (I == 23) {
                    dma16<0>(x, gx0, lb + XS + w * 1024);
                    if (w < 2) dma16<0>(x, gx1, lb + XS + 8192 + w * 1024);
                }
                if constexpr (I == 24) {
                    sdone = wall(sf0 == tag && sf1 == tag);
                    if (sdone) load_s();
                }
            };
            st2(IC<0>{}); st2(IC<1>{}); st2(IC<2>{}); st2(IC<3>{}); st2(IC<4>{}); st2(IC<5>{}); st2(IC<6>{}); st2(IC<7>{}); st2(IC<8>{});
            st2(IC<9>{}); st2(IC<10>{}); st2(IC<11>{}); st2(IC<12>{}); st2(IC<13>{}); st2(IC<14>{}); st2(IC<15>{}); st2(IC<16>{});
            st2(IC<17>{}); st2(IC<18>{}); st2(IC<19>{}); st2(IC<20>{}); st2(IC<21>{}); st2(IC<22>{}); st2(IC<23>{}); st2(IC<24>{});
            st2(IC<25>{}); st2(IC<26>{});
            {
                const bf16x8 bfrag = __builtin_bit_cast(bf16x8, af[26 & 7]);
#pragma unroll
                for (int d = 0; d < 5; ++d) a2[d] = mfma(wq[0][d], bfrag, a2[d]);
            }
            if (!sdone) {
                poll32(ws, sfp + l * 4, tag);
                poll32(ws, sfp + 256 + c16 * 4, tag);
                load_s();
            }
            char* ex = smem + 30720 + tbk * 5120 + l * 16;
            if (kp == 0) { *(f32x4*)(ex + 3072) = a2[3]; *(f32x4*)(ex + 4096) = a2[4]; }
            else { *(f32x4*)(ex) = a2[0]; *(f32x4*)(ex + 1024) = a2[1]; *(f32x4*)(ex + 2048) = a2[2]; }
            WAITBAR(0);
            auto fin = [&](int d, f32x4 o) {
                o += *(const f32x4*)(ex + d * 1024);
                char* xp = smem + XS + (16 * tbk + c16) * 160 + d * 32 + 8 * g;
                const u32x2 xv = *(const u32x2*)xp;
                const s16x4 sv4 = __builtin_amdgcn_ds_read_tr16_b64_v4i16(
                    (LDSP s16x4*)(smem + SS + (16 * d + 4 * g + (c16 >> 2)) * 128 + tbk * 32 + 8 * (l & 3)));
                const u32x2 sv = __builtin_bit_cast(u32x2, sv4);
                const float xs[4] = {lo16(xv.x), hi16(xv.x), lo16(xv.y), hi16(xv.y)};
                const float ss[4] = {lo16(sv.x), hi16(sv.x), lo16(sv.y), hi16(sv.y)};
                float r[4];
#pragma unroll
                for (int i = 0; i < 4; ++i) {
                    const float sg = __builtin_amdgcn_rcpf(1.f + __builtin_amdgcn_exp2f(-1.44269504f * o[i]));
                    r[i] = __builtin_fmaf(ss[i] * o[i], sg, xs[i]);
                }
                *(u32x2*)xp = u32x2{pk2(r[0], r[1]), pk2(r[2], r[3])};
            };
            if (kp == 0) { fin(0, a2[0]); fin(1, a2[1]); fin(2, a2[2]); }
            else { fin(3, a2[3]); fin(4, a2[4]); }
            BAR();
            *(u32x4*)((char*)out + gx0) = *(const u32x4*)(smem + XS + n0 * 16);
            if (w < 2) *(u32x4*)((char*)out + gx1) = *(const u32x4*)(smem + XS + n1 * 16);
        }
    }
}
#if defined(__HIP_DEVICE_COMPILE__)
#pragma clang attribute pop
#endif

extern "C" void kernel_launch(void* const* d_in, const int* in_sizes, int n_in, void* d_out, int out_size, void* d_ws,
                              size_t ws_size, hipStream_t stream) {
    (void)hipFuncSetAttribute((const void*)fused_kernel, hipFuncAttributeMaxDynamicSharedMemorySize, LDS_BYTES);
#define A(i) (const u16*)d_in[i]
    fused_kernel<<<512, 512, LDS_BYTES, stream>>>(A(0), A(1), A(2), A(3), A(4), A(5), A(6), A(7), A(8), A(10), A(11), A(12),
                                                  (u16*)d_out, (char*)d_ws);
}
```
